```python
import torch

def _softmax(x, axis=-1):
    return torch.softmax(x, dim=axis)
def _gelu(x):
    return torch.nn.functional.gelu(x, approximate="tanh")
def _silu(x):
    return torch.nn.functional.silu(x)
def _sigmoid(x):
    return torch.sigmoid(x)
def _layernorm(x, axis=-1, eps=1e-5):
    m = x.mean(dim=axis, keepdim=True)
    v = x.var(dim=axis, keepdim=True, unbiased=False)
    return (x - m) / torch.sqrt(v + eps)
def _scatter_add(tgt, idx, src, axis=0):
    return tgt.clone().index_add_(axis, idx, src)
def _segment_sum(src, seg, n):
    out = torch.zeros((n,) + src.shape[1:], dtype=src.dtype)
    return out.index_add_(0, seg, src)
def _pad(x, pw):
    flat = tuple(v for pair in reversed(pw) for v in pair)
    return torch.nn.functional.pad(x, flat)

kernel_name = 'parallel_sum__ssm_conv_h3__glu_gated_bilinear__prompt'

def setup_inputs():

    _w = (lambda a: (lambda t: t.to(torch.bfloat16) if t.is_floating_point() else t)(torch.as_tensor(a)))
    def _A_setup():
        B, S, D = 1, 2048, 576
        ks = 4
        return {
            "q": _w((lambda *s: torch.randn(*s, dtype=torch.bfloat16) * 0.05)(B, S, D)),
            "k": _w((lambda *s: torch.randn(*s, dtype=torch.bfloat16) * 0.2)(B, S, D)),
            "v": _w((lambda *s: torch.randn(*s, dtype=torch.bfloat16) * 0.2)(B, S, D)),
            "shift_k": _w((lambda *s: torch.randn(*s, dtype=torch.bfloat16) * 0.2)(D, ks) * 0.5),
            "ssm_k": _w((lambda *s: torch.randn(*s, dtype=torch.bfloat16) * 0.4)(D, S) * (1.0 / S ** 0.5)),
            "ssm_D": _w((lambda *s: torch.randn(*s, dtype=torch.bfloat16) * 0.2)(D)),
        }
    _A_in = _A_setup()

    def _B_setup():
        B, S, D, F = 1, 2048, 576, 4608
        return {
            "x": _w((lambda *s: torch.randn(*s, dtype=torch.bfloat16) * 0.05)(B, S, D)),
            "w_gate": _w((lambda *s: torch.randn(*s, dtype=torch.bfloat16) * 0.2)(D, F) * (D ** -0.5)),
            "w_up": _w((lambda *s: torch.randn(*s, dtype=torch.bfloat16) * 0.2)(D, F) * (D ** -0.5)),
            "w_down": _w((lambda *s: torch.randn(*s, dtype=torch.bfloat16) * 0.2)(F, D) * (F ** -0.5)),
        }
    _B_in = _B_setup()

    return {
        "A_q": _A_in["q"],
        "A_k": _A_in["k"],
        "A_v": _A_in["v"],
        "A_shift_k": _A_in["shift_k"],
        "A_ssm_k": _A_in["ssm_k"],
        "A_ssm_D": _A_in["ssm_D"],
        "B_x": _B_in["x"],
        "B_w_gate": _B_in["w_gate"],
        "B_w_up": _B_in["w_up"],
        "B_w_down": _B_in["w_down"],
    }

def reference(A_q, A_k, A_v, A_shift_k, A_ssm_k, A_ssm_D, B_x, B_w_gate, B_w_up, B_w_down):
    def _A_ref(q, k, v, shift_k, ssm_k, ssm_D):
        B, S, D = q.shape
        ks = shift_k.shape[-1]
        k_pad = _pad(k, ((0, 0), (ks - 1, 0), (0, 0)))
        k_shift = torch.zeros_like(k)
        for t in range(ks):
            k_shift = k_shift + k_pad[:, t:t + S, :] * shift_k[:, ks - 1 - t][None, None, :]
        kv = k_shift * v
        L = 2 * S
        kv_pad = _pad(kv, ((0, 0), (0, S), (0, 0)))
        kf_pad = _pad(ssm_k, ((0, 0), (0, S)))
        kv_f = (lambda x, axes=(-2,-1): torch.fft.rfft2(x.float(), dim=axes))(kv_pad[:, None, :, :], axes=(1, 2))
        kf_f = (lambda x, axes=(-2,-1): torch.fft.rfft2(x.float(), dim=axes))(kf_pad[:, None, :], axes=(1, 2))
        kf_f = (lambda x, *d: x.permute(*d))(kf_f, 1, 2, 0)
        y_f = kv_f * kf_f[None, :, :, :]
        y = (lambda x, s, axes=(-2,-1): torch.fft.irfft2(x, s=s, dim=axes).to(torch.bfloat16))(y_f, (1, L), axes=(1, 2))[:, 0, :S, :]
        y = y + kv * ssm_D[None, None, :]
        return q * y

    def _B_ref(x, w_gate, w_up, w_down):
        g = torch.einsum("bsd,df->bsf", x, w_gate)
        u = torch.einsum("bsd,df->bsf", x, w_up)
        h = g * u
        return torch.einsum("bsf,fd->bsd", h, w_down)

    x = A_q
    xn = _layernorm(x, axis=-1)
    return x + _A_ref(xn, A_k, A_v, A_shift_k, A_ssm_k, A_ssm_D) + _B_ref(xn, B_w_gate, B_w_up, B_w_down)
```

```cpp
#include <hip/hip_runtime.h>
#include <hip/hip_bf16.h>
#include <stdint.h>
#include <utility>

typedef unsigned short u16;
typedef unsigned int u32;
typedef short v4s __attribute__((ext_vector_type(4)));
typedef short v8s __attribute__((ext_vector_type(8)));
typedef float v4f __attribute__((ext_vector_type(4)));
typedef float v2f __attribute__((ext_vector_type(2)));
typedef u32 v4u __attribute__((ext_vector_type(4)));
typedef u32 v2u __attribute__((ext_vector_type(2)));
typedef __bf16 v2bf __attribute__((ext_vector_type(2)));
typedef __attribute__((address_space(3))) v4s* lds_v4s_ptr;

constexpr int S_ = 2048, D_ = 576, F_ = 4608;

#define DI __device__ __forceinline__

template <int... I, class Fn>
DI void static_for_impl(std::integer_sequence<int, I...>, Fn&& f) { (f(std::integral_constant<int, I>{}), ...); }
template <int N, class Fn>
DI void static_for(Fn&& f) { static_for_impl(std::make_integer_sequence<int, N>{}, f); }

DI float bf_lo(u32 w) { return __uint_as_float(w << 16); }
DI float bf_hi(u32 w) { return __uint_as_float(w & 0xffff0000u); }
DI float rbf(float f) { return (float)(__bf16)f; }
DI u32 pack_bf(float lo, float hi) {
  v2f t = {lo, hi};
  v2bf b = __builtin_convertvector(t, v2bf);
  return __builtin_bit_cast(u32, b);
}

#define STAMP(ws, kid, idx)
constexpr size_t WS_XNT = 0;
constexpr size_t WS_STATS = WS_XNT + (size_t)S_ * D_ * 2;
constexpr size_t WS_KVT = WS_STATS + (size_t)S_ * 8;
constexpr size_t WS_YT = WS_KVT + (size_t)S_ * D_ * 2;
constexpr size_t WS_HT = WS_YT + (size_t)S_ * D_ * 2;
constexpr size_t WS_PART = WS_HT + (size_t)S_ * F_ * 2;

__global__ __launch_bounds__(64) void pre_kernel(const u16* __restrict__ x, const u16* __restrict__ kin,
                                                 const u16* __restrict__ vin, const u16* __restrict__ shk,
                                                 char* __restrict__ ws) {
  const int lane = threadIdx.x;
  STAMP(ws, 0, 0);
  if (blockIdx.x < 256) {
    const int r = lane >> 3, c = lane & 7;
    const int row = blockIdx.x * 8 + r;
    const v4u* src = (const v4u*)(x + (size_t)row * D_);
    v4u d[9];
#pragma unroll
    for (int i = 0; i < 9; ++i) d[i] = src[c + 8 * i];
    float s = 0.f;
#pragma unroll
    for (int i = 0; i < 9; ++i)
#pragma unroll
      for (int j = 0; j < 4; ++j) s += bf_lo(d[i][j]) + bf_hi(d[i][j]);
    s += __shfl_xor(s, 1); s += __shfl_xor(s, 2); s += __shfl_xor(s, 4);
    const float mean = s * (1.0f / D_);
    float q = 0.f;
#pragma unroll
    for (int i = 0; i < 9; ++i)
#pragma unroll
      for (int j = 0; j < 4; ++j) {
        float a = bf_lo(d[i][j]) - mean, b = bf_hi(d[i][j]) - mean;
        q += a * a + b * b;
      }
    q += __shfl_xor(q, 1); q += __shfl_xor(q, 2); q += __shfl_xor(q, 4);
    const float var = q * (1.0f / D_);
    const float mb = rbf(mean);
    const float sd = rbf(sqrtf(rbf(rbf(var) + 1e-5f)));
    if (c == 0) ((v2f*)(ws + WS_STATS))[row] = v2f{mb, sd};
    char* xnt = ws + WS_XNT;
    const int tb = row >> 4, m = row & 15;
    const int cc = c & 3, T = cc >> 1, q0 = 2 * (cc & 1);
#pragma unroll
    for (int i = 0; i < 9; ++i) {
      u32 o[4];
#pragma unroll
      for (int j = 0; j < 4; ++j) {
        float a = rbf(bf_lo(d[i][j]) - mb) / sd;
        float b = rbf(bf_hi(d[i][j]) - mb) / sd;
        o[j] = pack_bf(a, b);
      }
      const int kb = 2 * i + (c >> 2);
      char* base = xnt + ((size_t)(tb * 18 + kb) * 64 + (q0 * 16 + m)) * 16 + 8 * T;
      *(v2u*)(base) = v2u{o[0], o[1]};
      *(v2u*)(base + 256) = v2u{o[2], o[3]};
    }
  } else {
    const int b = blockIdx.x - 256;
    const int tt = b / 9, ct = b % 9;
    const int tg = lane >> 4, cx = lane & 15;
    const int s0 = tt * 32 + tg * 8, d0 = ct * 64 + cx * 4;
    v2u kk[11], vv[8];
#pragma unroll
    for (int i = 0; i < 11; ++i) {
      const int s = s0 - 3 + i;
      kk[i] = (s >= 0) ? *(const v2u*)(kin + (size_t)s * D_ + d0) : v2u{0u, 0u};
    }
#pragma unroll
    for (int i = 0; i < 8; ++i) vv[i] = *(const v2u*)(vin + (size_t)(s0 + i) * D_ + d0);
    const v4u w0 = *(const v4u*)(shk + (size_t)d0 * 4);
    const v4u w1 = *(const v4u*)(shk + (size_t)d0 * 4 + 8);
    u16* kvt = (u16*)(ws + WS_KVT);
#pragma unroll
    for (int ch = 0; ch < 4; ++ch) {
      const u32 wa = ch == 0 ? w0[0] : ch == 1 ? w0[2] : ch == 2 ? w1[0] : w1[2];
      const u32 wb = ch == 0 ? w0[1] : ch == 1 ? w0[3] : ch == 2 ? w1[1] : w1[3];
      const float t0 = bf_lo(wa), t1 = bf_hi(wa), t2 = bf_lo(wb), t3 = bf_hi(wb);
      float kf[11];
#pragma unroll
      for (int i = 0; i < 11; ++i) kf[i] = (ch & 1) ? bf_hi(kk[i][ch >> 1]) : bf_lo(kk[i][ch >> 1]);
      float r[8];
#pragma unroll
      for (int i = 0; i < 8; ++i) {
        float a = rbf(kf[i] * t3);
        a = rbf(a + rbf(kf[i + 1] * t2));
        a = rbf(a + rbf(kf[i + 2] * t1));
        a = rbf(a + rbf(kf[i + 3] * t0));
        const float vf = (ch & 1) ? bf_hi(vv[i][ch >> 1]) : bf_lo(vv[i][ch >> 1]);
        r[i] = a * vf;
      }
      v4u o = {pack_bf(r[7], r[6]), pack_bf(r[5], r[4]), pack_bf(r[3], r[2]), pack_bf(r[1], r[0])};
      *(v4u*)(kvt + (size_t)(d0 + ch) * S_ + s0) = o;
    }
  }
  STAMP(ws, 0, 1);
}

constexpr int G_STAGE = 34816, G_NBUF = 4, G_LDS = G_STAGE * G_NBUF;
constexpr int G_XOFF = 18432;

typedef __attribute__((address_space(3))) v8s* lds_v8s_ptr;
typedef __attribute__((address_space(3))) char* lds_ptr;
DI v8s tr_frag(u32 p, int off_lo, int off_hi) {
  v4s a = __builtin_amdgcn_ds_read_tr16_b64_v4i16((lds_v4s_ptr)((lds_ptr)p + off_lo));
  v4s b = __builtin_amdgcn_ds_read_tr16_b64_v4i16((lds_v4s_ptr)((lds_ptr)p + off_hi));
  return __builtin_shufflevector(a, b, 0, 1, 2, 3, 4, 5, 6, 7);
}
DI v8s ld_frag(u32 p, int off) { return *(lds_v8s_ptr)((lds_ptr)p + off); }
DI u32 opaque(u32 v) { asm volatile("" : "+v"(v)); return v; }
DI u32 lds_addr(const void* p) { return (u32)(size_t)(lds_ptr)p; }
typedef __amdgpu_buffer_rsrc_t srd_t;
DI srd_t make_srd(const void* p, u32 bytes) {
  return __builtin_amdgcn_make_buffer_rsrc((void*)p, 0, bytes, 0x00020000);
}
DI void dma16(u32 m0v, u32 voff, srd_t srd, u32 soff) {
  asm volatile("s_mov_b32 m0, %0\n\ts_nop 0\n\tbuffer_load_dwordx4 %1, %2, %3 offen lds"
               :: "s"(m0v), "v"(voff), "s"(srd), "s"(soff) : "memory");
}
#define SCHED0() __builtin_amdgcn_sched_barrier(0)
DI void mfma16(v4f& c, const v8s& a, const v8s& b) {
  asm volatile("v_mfma_f32_16x16x32_bf16 %0, %1, %2, %0" : "+v"(c) : "v"(a), "v"(b));
}
DI void mfma16_first(v4f& c, const v8s& a, const v8s& b) {
  asm volatile("v_mfma_f32_16x16x32_bf16 %0, %1, %2, 0" : "=&v"(c) : "v"(a), "v"(b));
}
DI void mfma_drain() { asm volatile("s_nop 15\n\ts_nop 15" ::: "memory"); }
template <int N> DI void wait_vm() { asm volatile("s_waitcnt vmcnt(%0)" :: "n"(N) : "memory"); }
DI void stage_barrier() { asm volatile("s_waitcnt lgkmcnt(0)\n\ts_barrier" ::: "memory"); }

template <int NT>
DI void gemm1_body(u32 sb, srd_t sg, srd_t su, srd_t ss, srd_t sx, u32 wsoff, u32 ssoff, u32 xsoff,
                   int w, int lane, int wm, char* hdst, int kb0, int kbs, int shalf, int tb0) {
  constexpr int NU = 2 * NT;
  v4f acc[2][NT][4];
  STAMP(hdst - WS_HT, 1, 0);

  const int g = lane >> 4, qq = (lane & 15) >> 2, p = lane & 3;
  const int rr = 4 * (g & 1) + qq;
  const int wl0 = rr * 128 + (2 * (rr >> 1)) * 16 + p * 8;
  u32 wp[2][4], wsg[2], xp[2];
#pragma unroll
  for (int t = 0; t < 4; ++t) {
    wp[0][t] = opaque((sb + wl0 + (g >> 1) * 4096 + (NT == 5 ? 1024 : 0)) ^ (t << 5));
    wp[1][t] = opaque(wp[0][t] + 2 * G_STAGE);
  }
  wsg[0] = opaque(sb + 16384 + rr * 32 + p * 8 + (g >> 1) * 256);    wsg[1] = opaque(wsg[0] + 2 * G_STAGE);
  xp[0] = opaque(sb + G_XOFF + wm * 4096 + lane * 16);               xp[1] = opaque(xp[0] + 2 * G_STAGE);
  const u32 woff = (u32)((lane >> 3) * (F_ * 2) + ((lane & 7) ^ (2 * (lane >> 4))) * 16);
  const u32 soff = (u32)(((lane >> 1) & 7) * (F_ * 2) + (lane & 1) * 16);
  const u32 xoff = lane * 16;
  const u32 m0w = sb + (w & 3) * 4096 + (w >> 2) * 2048;
  const u32 m0s = sb + 16384 + (w >> 2) * 1024 + (w & 3) * 256;
  const u32 m0x = sb + G_XOFF + w * 2048;

  auto dma_piece = [&](int st, int i) {
    const u32 bo = (st & 3) * G_STAGE;
    const u32 ko = (u32)st * (32 * F_ * 2);
    if (i == 0) dma16(m0w + bo, woff, sg, wsoff + ko);
    else if (i == 1) dma16(m0w + bo + 1024, woff, sg, wsoff + ko + 128);
    else if (i == 2) { if (lane < 16) dma16(m0s + bo, soff, sg, ssoff + ko); }
    else if (i == 3) dma16(m0x + bo, xoff, sx, xsoff + st * 1024);
    else dma16(m0x + bo + 1024, xoff, sx, xsoff + st * 1024 + 18 * 1024);
  };
  auto rdW = [&](int st, int u) -> v8s {
    const int T = u >> 1, gu = u & 1;
    const int bo = (st & 1) * G_STAGE;
    if (T < 4) { const int o = bo + gu * 2048; return tr_frag(wp[(st >> 1) & 1][T], o, o + 2 * 4096); }
    else { const int o = bo + gu * 1024; return tr_frag(wsg[(st >> 1) & 1], o, o + 2 * 256); }
  };
  v8s wf[3], xf[2][4];
#pragma unroll
  for (int st = 0; st < 3; ++st)
#pragma unroll
    for (int i = 0; i < 5; ++i) dma_piece(st, i);
  wait_vm<10>();
  stage_barrier();
#pragma unroll
  for (int mb = 0; mb < 4; ++mb) xf[0][mb] = ld_frag(xp[0], mb * 1024);
  STAMP(hdst - WS_HT, 1, 1);
  wf[0] = rdW(0, 0);
  wf[1] = rdW(0, 1);
  dma_piece(3, 0); dma_piece(3, 1);
  SCHED0();
  static_for<18 * NU>([&](auto UC) {
    constexpr int U = decltype(UC)::value, st = U / NU, u = U % NU;
    if constexpr (u == NU - 2 && st + 1 < 18) {
      if constexpr (st + 1 <= 15) wait_vm<10>(); else if constexpr (st + 1 == 16) wait_vm<5>(); else wait_vm<0>();
      stage_barrier();
#pragma unroll
      for (int mb = 0; mb < 4; ++mb)
        xf[(st + 1) & 1][mb] = ld_frag(xp[((st + 1) >> 1) & 1], ((st + 1) & 1) * G_STAGE + mb * 1024);
    }
    if constexpr (u + 2 < NU) wf[(U + 2) % 3] = rdW(st, u + 2);
    else if constexpr (st + 1 < 18) wf[(U + 2) % 3] = rdW(st + 1, u + 2 - NU);
    if constexpr (u >= NU - 2) { if constexpr (st + 4 < 18) dma_piece(st + 4, u - (NU - 2)); }
    else if constexpr (u < 3) { if constexpr (st + 3 < 18) dma_piece(st + 3, u + 2); }
    SCHED0();
    constexpr int T = u >> 1, gu = u & 1;
#pragma unroll
    for (int mb = 0; mb < 4; ++mb) {
      if constexpr (st == 0) mfma16_first(acc[gu][T][mb], wf[U % 3], xf[st & 1][mb]);
      else mfma16(acc[gu][T][mb], wf[U % 3], xf[st & 1][mb]);
    }
    SCHED0();
  });
  mfma_drain();
  SCHED0();
  STAMP(hdst - WS_HT, 1, 2);
#pragma unroll
  for (int mb = 0; mb < 4; ++mb) {
    char* hrow = hdst + (size_t)(tb0 + mb) * (144 * 1024) + lane * 16;
    u32 hv[NT][2];
#pragma unroll
    for (int T = 0; T < NT; ++T) {
      float hh[4];
#pragma unroll
      for (int r = 0; r < 4; ++r) hh[r] = rbf(acc[0][T][mb][r]) * rbf(acc[1][T][mb][r]);
      hv[T][0] = pack_bf(hh[0], hh[1]);
      hv[T][1] = pack_bf(hh[2], hh[3]);
    }
    *(v4u*)(hrow + (size_t)kb0 * 1024) = v4u{hv[0][0], hv[0][1], hv[1][0], hv[1][1]};
    *(v4u*)(hrow + (size_t)(kb0 + 1) * 1024) = v4u{hv[2][0], hv[2][1], hv[3][0], hv[3][1]};
    if (NT == 5) *(v2u*)(hrow + (size_t)kbs * 1024 + 8 * shalf) = v2u{hv[NT - 1][0], hv[NT - 1][1]};
  }
  STAMP(hdst - WS_HT, 1, 3);
}

__global__ __launch_bounds__(512, 2) void gemm1_kernel(const u16* __restrict__ Wg, const u16* __restrict__ Wu,
                                                       char* __restrict__ ws) {
  extern __shared__ __attribute__((aligned(1024))) char smem[];
  const int bid = blockIdx.x;
  const int xcd = bid & 7, idx = bid >> 3;
  const int tile_n = xcd * 4 + (idx & 3), tile_m = idx >> 2;
  const int tid = threadIdx.x, lane = tid & 63;
  const int w = __builtin_amdgcn_readfirstlane(tid >> 6);
  const int wm = w & 3, wn = w >> 2;
  const int odd = tile_n & 1;
  const int rc = 144 * tile_n + (odd ? 16 : 0);
  const int sc = 144 * tile_n + (odd ? 0 : 128);
  const srd_t sw = make_srd((w >> 2) ? Wu : Wg, D_ * F_ * 2);
  const srd_t sx = make_srd(ws + WS_XNT, S_ * D_ * 2);
  const u32 wsoff = (u32)((8 * (w & 3)) * (F_ * 2) + rc * 2);
  const u32 ssoff = (u32)((8 * (w & 3)) * (F_ * 2) + sc * 2);
  const u32 xsoff = (u32)((tile_m * 16 + 2 * w) * (18 * 1024));
  char* hdst = ws + WS_HT;
  const int tb0 = tile_m * 16 + wm * 4;
  const u32 sb = lds_addr(smem);
  if (wn == 0)
    gemm1_body<5>(sb, sw, sw, sw, sx, wsoff, ssoff, xsoff, w, lane, wm, hdst, (rc >> 5) + 2, sc >> 5, (sc >> 4) & 1, tb0);
  else
    gemm1_body<4>(sb, sw, sw, sw, sx, wsoff, ssoff, xsoff, w, lane, wm, hdst, (rc >> 5), 0, 0, tb0);
}

template <int NT>
DI void gemm2_body(char* smem, u32 sb, srd_t sw, srd_t sx, u32 wsoff, u32 xsoff, int w, int lane, int kg, int wm,
                   int tbase, float* pdst, char* ws) {
  constexpr int NU = NT;
  v4f acc[NT][4];
  STAMP(ws, 2, 0);

  const int g = lane >> 4, qq = (lane & 15) >> 2, p = lane & 3;
  const int rr = 4 * (g & 1) + qq;
  const int wl0 = rr * 128 + (2 * (rr >> 1)) * 16 + p * 8;
  u32 wp[2][4], wsg[2], xp[2];
#pragma unroll
  for (int t = 0; t < 4; ++t) {
    wp[0][t] = opaque((sb + wl0 + ((g >> 1) + 4 * kg) * 2304 + (NT == 5 ? 1024 : 0)) ^ (t << 5));
    wp[1][t] = opaque(wp[0][t] + 2 * G_STAGE);
  }
  wsg[0] = opaque(sb + 2048 + rr * 32 + p * 8 + ((g >> 1) + 4 * kg) * 2304); wsg[1] = opaque(wsg[0] + 2 * G_STAGE);
  xp[0] = opaque(sb + G_XOFF + wm * 8192 + kg * 1024 + lane * 16); xp[1] = opaque(xp[0] + 2 * G_STAGE);
  const u32 woff = (u32)((lane >> 3) * (D_ * 2) + ((lane & 7) ^ (2 * (lane >> 4))) * 16);
  const u32 soff = (u32)(((lane >> 1) & 7) * (D_ * 2) + (lane & 1) * 16);
  const u32 xoff = lane * 16;
  const u32 m0w = sb + w * 2304;
  const u32 m0x = sb + G_XOFF + w * 2048;

  auto dma_piece = [&](int st, int i) {
    const u32 bo = (st & 3) * G_STAGE;
    const u32 ko = (u32)st * (64 * D_ * 2);
    if (i == 0) dma16(m0w + bo, woff, sw, wsoff + ko);
    else if (i == 1) dma16(m0w + bo + 1024, woff, sw, wsoff + ko + 128);
    else if (i == 2) { if (lane < 16) dma16(m0w + bo + 2048, soff, sw, wsoff + ko + 256); }
    else if (i == 3) dma16(m0x + bo, xoff, sx, xsoff + st * 2048);
    else dma16(m0x + bo + 1024, xoff, sx, xsoff + st * 2048 + 1024);
  };
  auto rdW = [&](int st, int u) -> v8s {
    const int o = (st & 1) * G_STAGE;
    if (u < 4) return tr_frag(wp[(st >> 1) & 1][u], o, o + 2 * 2304);
    else return tr_frag(wsg[(st >> 1) & 1], o, o + 2 * 2304);
  };
  v8s wf[3], xf[2][4];
#pragma unroll
  for (int st = 0; st < 3; ++st)
#pragma unroll
    for (int i = 0; i < 5; ++i) dma_piece(st, i);
  wait_vm<10>();
  stage_barrier();
#pragma unroll
  for (int mb = 0; mb < 4; ++mb) xf[0][mb] = ld_frag(xp[0], mb * 2048);
  STAMP(ws, 2, 1);
  wf[0] = rdW(0, 0);
  wf[1] = rdW(0, 1);
  dma_piece(3, 0); dma_piece(3, 1);
  SCHED0();
  static_for<18 * NU>([&](auto UC) {
    constexpr int U = decltype(UC)::value, st = U / NU, u = U % NU;
    if constexpr (u == NU - 2 && st + 1 < 18) {
      if constexpr (st + 1 <= 15) wait_vm<10>(); else if constexpr (st + 1 == 16) wait_vm<5>(); else wait_vm<0>();
      stage_barrier();
#pragma unroll
      for (int mb = 0; mb < 4; ++mb)
        xf[(st + 1) & 1][mb] = ld_frag(xp[((st + 1) >> 1) & 1], ((st + 1) & 1) * G_STAGE + mb * 2048);
    }
    if constexpr (u + 2 < NU) wf[(U + 2) % 3] = rdW(st, u + 2);
    else if constexpr (st + 1 < 18) wf[(U + 2) % 3] = rdW(st + 1, u + 2 - NU);
    if constexpr (u >= NU - 2) { if constexpr (st + 4 < 18) dma_piece(st + 4, u - (NU - 2)); }
    else if constexpr (u == 0) { if constexpr (st + 3 < 18) { dma_piece(st + 3, 2); dma_piece(st + 3, 3); } }
    else if constexpr (u == 1) { if constexpr (st + 3 < 18) dma_piece(st + 3, 4); }
    SCHED0();
#pragma unroll
    for (int mb = 0; mb < 4; ++mb) {
      if constexpr (st == 0) mfma16_first(acc[u][mb], wf[U % 3], xf[st & 1][mb]);
      else mfma16(acc[u][mb], wf[U % 3], xf[st & 1][mb]);
    }
    SCHED0();
  });
  mfma_drain();
  SCHED0();
  STAMP(ws, 2, 2);
  __syncthreads();
  char* red = smem + ((wm * 9 + tbase) * 4) * 1024 + lane * 16;
  if (kg == 1) {
#pragma unroll
    for (int T = 0; T < NT; ++T)
#pragma unroll
      for (int mb = 0; mb < 4; ++mb) *(v4f*)(red + (T * 4 + mb) * 1024) = acc[T][mb];
  }
  __syncthreads();
  if (kg == 0) {
    const int m = lane & 15, q = lane >> 4;
#pragma unroll
    for (int T = 0; T < NT; ++T)
#pragma unroll
      for (int mb = 0; mb < 4; ++mb) {
        v4f o = acc[T][mb] + *(const v4f*)(red + (T * 4 + mb) * 1024);
        *(v4f*)(pdst + (size_t)(wm * 64 + mb * 16 + m) * D_ + (tbase + T) * 16 + q * 4) = o;
      }
  }
  STAMP(ws, 2, 3);
}

__global__ __launch_bounds__(512, 2) void gemm2_kernel(const u16* __restrict__ Wd, char* __restrict__ ws) {
  extern __shared__ __attribute__((aligned(1024))) char smem[];
  const int bid = blockIdx.x;
  const int xcd = bid & 7, idx = bid >> 3;
  const int ksl = xcd >> 1;
  const int tl = (xcd & 1) * 32 + idx;
  const int tile_m = tl >> 2, tile_n = tl & 3;
  const int tid = threadIdx.x, lane = tid & 63;
  const int w = __builtin_amdgcn_readfirstlane(tid >> 6);
  const int kg = w >> 2, wm = (w >> 1) & 1, wn = (w & 1) ^ kg;
  const srd_t sw = make_srd(Wd, D_ * F_ * 2);
  const srd_t sx = make_srd(ws + WS_HT, S_ * F_ * 2);
  const u32 wsoff = (u32)((ksl * 1152 + 8 * w) * (D_ * 2) + tile_n * 288);
  const u32 xsoff = (u32)(((tile_m * 8 + w) * 144 + ksl * 36) * 1024);
  float* pdst = (float*)(ws + WS_PART) + ((size_t)ksl * S_ + tile_m * 128) * D_ + tile_n * 144;
  const u32 sb = lds_addr(smem);
  if (wn == 0)
    gemm2_body<5>(smem, sb, sw, sx, wsoff, xsoff, w, lane, kg, wm, 4, pdst, ws);
  else
    gemm2_body<4>(smem, sb, sw, sx, wsoff, xsoff, w, lane, kg, wm, 0, pdst, ws);
}

constexpr int C_KBYTES = 2080 * 8, C_XBYTES = 8 * 768, C_WAVE = C_KBYTES + C_XBYTES;

__global__ __launch_bounds__(256) void conv_kernel(const u16* __restrict__ ssmk, const u16* __restrict__ ssmD,
                                                   char* __restrict__ ws) {
  extern __shared__ __attribute__((aligned(1024))) char smem[];
  const int tid = threadIdx.x, lane = tid & 63;
  const int w = __builtin_amdgcn_readfirstlane(tid >> 6);
  const int ch = blockIdx.x * 4 + w;
  char* kl = smem + w * C_WAVE;
  char* xl = kl + C_KBYTES;
  STAMP(ws, 3, 0);
  const u32* kr = (const u32*)(ssmk + (size_t)ch * S_);
#pragma unroll
  for (int it = 0; it < 17; ++it) {
    const int L = it * 64 + lane;
    const int dw = L - 16;
    u32 d0 = 0, d1 = 0, d2 = 0;
    if (dw >= 0 && dw < 1024) d0 = kr[dw];
    if (dw + 1 >= 0 && dw + 1 < 1024) d1 = kr[dw + 1];
    if (dw + 2 >= 0 && dw + 2 < 1024) d2 = kr[dw + 2];
    v4u o = {d0, d1, __builtin_amdgcn_alignbit(d1, d0, 16), __builtin_amdgcn_alignbit(d2, d1, 16)};
    if (L < 1040) *(v4u*)(kl + L * 16) = o;
  }
  const v4u* xs = (const v4u*)(ws + WS_KVT + (size_t)ch * S_ * 2);
#pragma unroll
  for (int it = 0; it < 4; ++it) {
    const int cidx = it * 64 + lane;
    v4u d = xs[cidx];
    *(v4u*)(xl + (cidx & 7) * 768 + ((cidx >> 3) + 16) * 16) = d;
  }
#pragma unroll
  for (int it = 0; it < 2; ++it) {
    const int z = it * 64 + lane;
    *(v4u*)(xl + (z >> 4) * 768 + (z & 15) * 16) = v4u{0u, 0u, 0u, 0u};
  }
  __builtin_amdgcn_s_waitcnt(0xC07F);
  __builtin_amdgcn_wave_barrier();
  STAMP(ws, 3, 1);

  const int i16 = lane & 15, q = lane >> 4;
  const char* ap = kl + (i16 - 8 * q - 7 + 32) * 8;
  const char* bp = xl + q * 768 + (i16 + 16) * 16;
  v4f acc[4][2];
#pragma unroll
  for (int a = 0; a < 4; ++a) { acc[a][0] = v4f{0.f, 0.f, 0.f, 0.f}; acc[a][1] = v4f{0.f, 0.f, 0.f, 0.f}; }
  v8s win[6];
#pragma unroll
  for (int a = 0; a < 6; ++a) win[a] = v8s{0, 0, 0, 0, 0, 0, 0, 0};
#pragma unroll
  for (int c = 0; c < 32; ++c) {
    win[0] = win[4]; win[1] = win[5];
#pragma unroll
    for (int j = 0; j < 4; ++j) {
      const int delta = 64 * c + 16 * j;
      v4s lo = *(const v4s*)(ap + 8 * delta);
      v4s hi = *(const v4s*)(ap + 8 * delta + 32);
      win[2 + j] = __builtin_shufflevector(lo, hi, 0, 1, 2, 3, 4, 5, 6, 7);
    }
#pragma unroll
    for (int n = 1; n >= 0; --n) {
      if (n == 0 && c >= 16) continue;
#pragma unroll
      for (int kb = 0; kb < 2; ++kb) {
        v8s bf = *(const v8s*)(bp + 4 * kb * 768 + 16 * (16 * n - c));
#pragma unroll
        for (int ib = 0; ib < 4; ++ib) {
          if (c == 0 && ib - 2 * kb < 0) continue;
          acc[ib][n] = __builtin_amdgcn_mfma_f32_16x16x32_bf16(win[2 + ib - 2 * kb], bf, acc[ib][n], 0, 0, 0);
        }
      }
    }
  }
  STAMP(ws, 3, 2);
  const float Dd = bf_lo((u32)ssmD[ch]);
  u16* yt = (u16*)(ws + WS_YT) + (size_t)ch * S_;
#pragma unroll
  for (int n = 0; n < 2; ++n)
#pragma unroll
    for (int ib = 0; ib < 4; ++ib) {
      const int beta = 16 * n + i16;
      const v2u kvp = *(const v2u*)(xl + (2 * ib + (q >> 1)) * 768 + (beta + 16) * 16 + 8 * (1 - (q & 1)));
      const float k3 = bf_lo(kvp[0]), k2 = bf_hi(kvp[0]), k1 = bf_lo(kvp[1]), k0 = bf_hi(kvp[1]);
      const float y0 = rbf(acc[ib][n][0]) + rbf(k0 * Dd);
      const float y1 = rbf(acc[ib][n][1]) + rbf(k1 * Dd);
      const float y2 = rbf(acc[ib][n][2]) + rbf(k2 * Dd);
      const float y3 = rbf(acc[ib][n][3]) + rbf(k3 * Dd);
      *(v2u*)(yt + 64 * beta + 16 * ib + 4 * q) = v2u{pack_bf(y0, y1), pack_bf(y2, y3)};
    }
  STAMP(ws, 3, 3);
}

__global__ __launch_bounds__(256) void final_kernel(const u16* __restrict__ x, const char* __restrict__ ws,
                                                    u16* __restrict__ out) {
  __shared__ __attribute__((aligned(16))) u16 ys[144][40];
  const int tid = threadIdx.x;
  STAMP((char*)ws, 4, 0);
  const int tm = blockIdx.x >> 2, tn = blockIdx.x & 3;
  const int s0 = tm * 32, d0 = tn * 144;
  const u16* yt = (const u16*)(ws + WS_YT);
  for (int i = tid; i < 144 * 4; i += 256) {
    const int d = i >> 2, c = i & 3;
    *(v4u*)(&ys[d][c * 8]) = *(const v4u*)(yt + (size_t)(d0 + d) * S_ + s0 + c * 8);
  }
  __syncthreads();
  STAMP((char*)ws, 4, 1);
  const float* part = (const float*)(ws + WS_PART);
  for (int i = tid; i < 32 * 18; i += 256) {
    const int sl = i / 18, c = i % 18;
    const int s = s0 + sl, d = d0 + c * 8;
    v4f a0 = {0.f, 0.f, 0.f, 0.f}, a1 = {0.f, 0.f, 0.f, 0.f};
#pragma unroll
    for (int k = 0; k < 4; ++k) {
      const float* pp = part + ((size_t)k * S_ + s) * D_ + d;
      a0 += *(const v4f*)pp;
      a1 += *(const v4f*)(pp + 4);
    }
    const v4u xv = *(const v4u*)(x + (size_t)s * D_ + d);
    const v2f st = ((const v2f*)(ws + WS_STATS))[s];
    float o[8];
#pragma unroll
    for (int j = 0; j < 8; ++j) {
      const float xf = (j & 1) ? bf_hi(xv[j >> 1]) : bf_lo(xv[j >> 1]);
      const float xn = rbf(rbf(xf - st[0]) / st[1]);
      const float y = bf_lo((u32)ys[c * 8 + j][sl]);
      const float A = rbf(xn * y);
      const float Bv = rbf(j < 4 ? a0[j] : a1[j - 4]);
      o[j] = rbf(xf + A) + Bv;
    }
    *(v4u*)(out + (size_t)s * D_ + d) =
        v4u{pack_bf(o[0], o[1]), pack_bf(o[2], o[3]), pack_bf(o[4], o[5]), pack_bf(o[6], o[7])};
  }
  STAMP((char*)ws, 4, 2);
}

extern "C" void kernel_launch(void* const* d_in, const int* in_sizes, int n_in, void* d_out, int out_size,
                              void* d_ws, size_t ws_size, hipStream_t stream) {
  const u16* q = (const u16*)d_in[0];
  const u16* k = (const u16*)d_in[1];
  const u16* v = (const u16*)d_in[2];
  const u16* shk = (const u16*)d_in[3];
  const u16* ssmk = (const u16*)d_in[4];
  const u16* ssmD = (const u16*)d_in[5];
  const u16* Wg = (const u16*)d_in[7];
  const u16* Wu = (const u16*)d_in[8];
  const u16* Wd = (const u16*)d_in[9];
  char* ws = (char*)d_ws;
  (void)hipFuncSetAttribute((const void*)gemm1_kernel, hipFuncAttributeMaxDynamicSharedMemorySize, G_LDS);
  (void)hipFuncSetAttribute((const void*)gemm2_kernel, hipFuncAttributeMaxDynamicSharedMemorySize, G_LDS);
  (void)hipFuncSetAttribute((const void*)conv_kernel, hipFuncAttributeMaxDynamicSharedMemorySize, 4 * C_WAVE);
  pre_kernel<<<256 + 576, 64, 0, stream>>>(q, k, v, shk, ws);
  conv_kernel<<<144, 256, 4 * C_WAVE, stream>>>(ssmk, ssmD, ws);
  gemm1_kernel<<<256, 512, G_LDS, stream>>>(Wg, Wu, ws);
  gemm2_kernel<<<256, 512, G_LDS, stream>>>(Wd, ws);
  final_kernel<<<256, 256, 0, stream>>>(q, ws, (u16*)d_out);
}
```

```cpp
#include <hip/hip_runtime.h>
#include <hip/hip_bf16.h>
#include <stdint.h>
#include <utility>

typedef unsigned short u16;
typedef unsigned int u32;
typedef short v4s __attribute__((ext_vector_type(4)));
typedef short v8s __attribute__((ext_vector_type(8)));
typedef float v4f __attribute__((ext_vector_type(4)));
typedef float v2f __attribute__((ext_vector_type(2)));
typedef u32 v4u __attribute__((ext_vector_type(4)));
typedef u32 v2u __attribute__((ext_vector_type(2)));
typedef __bf16 v2bf __attribute__((ext_vector_type(2)));
typedef __attribute__((address_space(3))) v4s* lds_v4s_ptr;

constexpr int S_ = 2048, D_ = 576, F_ = 4608;

#define DI __device__ __forceinline__

template <int... I, class Fn>
DI void static_for_impl(std::integer_sequence<int, I...>, Fn&& f) { (f(std::integral_constant<int, I>{}), ...); }
template <int N, class Fn>
DI void static_for(Fn&& f) { static_for_impl(std::make_integer_sequence<int, N>{}, f); }

DI float bf_lo(u32 w) { return __uint_as_float(w << 16); }
DI float bf_hi(u32 w) { return __uint_as_float(w & 0xffff0000u); }
DI float rbf(float f) { return (float)(__bf16)f; }
DI u32 pack_bf(float lo, float hi) {
  v2f t = {lo, hi};
  v2bf b = __builtin_convertvector(t, v2bf);
  return __builtin_bit_cast(u32, b);
}

#define STAMP(ws, kid, idx)
#define FS_DECL
#define FSTAMP(i)
#define FS_DUMP(ws, n)
constexpr size_t WS_XNT = 0;
constexpr size_t WS_STATS = WS_XNT + (size_t)S_ * D_ * 2;
constexpr size_t WS_KVT = WS_STATS + (size_t)S_ * 8;
constexpr size_t WS_YT = WS_KVT + (size_t)S_ * D_ * 2;
constexpr size_t WS_HT = WS_YT + (size_t)S_ * D_ * 2;
constexpr size_t WS_PART = WS_HT + (size_t)S_ * F_ * 2;
constexpr size_t WS_CNT = WS_PART + (size_t)64 * 4 * 8 * 4 * 3072;
constexpr size_t WS_HCNT = WS_CNT + 2048 * 64;
constexpr size_t WS_YCNT = WS_HCNT + 3 * 64 * 128;

__global__ __launch_bounds__(256) void pre_kernel(const u16* __restrict__ x, const u16* __restrict__ kin,
                                                  const u16* __restrict__ vin, const u16* __restrict__ shk,
                                                  char* __restrict__ ws) {
  const int lane = threadIdx.x & 63, w = threadIdx.x >> 6;
  STAMP(ws, 0, 0);
  if (blockIdx.x < 8) *(v4u*)(ws + WS_CNT + (size_t)(blockIdx.x * 256 + threadIdx.x) * 64) = v4u{0u, 0u, 0u, 0u};
  else if (blockIdx.x < 15) *(v4u*)(ws + WS_HCNT + (size_t)((blockIdx.x - 8) * 256 + threadIdx.x) * 16) = v4u{0u, 0u, 0u, 0u};
  if (blockIdx.x < 128) {
    const int r = lane >> 4, c = lane & 15;
    const int row = blockIdx.x * 16 + w * 4 + r;
    const v4u* src = (const v4u*)(x + (size_t)row * D_);
    v4u d[5];
#pragma unroll
    for (int i = 0; i < 4; ++i) d[i] = src[c + 16 * i];
    d[4] = src[64 + (c & 7)];
    const float lastw = (c < 8) ? 1.f : 0.f;
    float s = 0.f, s4 = 0.f;
#pragma unroll
    for (int i = 0; i < 4; ++i)
#pragma unroll
      for (int j = 0; j < 4; ++j) s += bf_lo(d[i][j]) + bf_hi(d[i][j]);
#pragma unroll
    for (int j = 0; j < 4; ++j) s4 += bf_lo(d[4][j]) + bf_hi(d[4][j]);
    s += s4 * lastw;
    s += __shfl_xor(s, 1); s += __shfl_xor(s, 2); s += __shfl_xor(s, 4); s += __shfl_xor(s, 8);
    const float mean = s * (1.0f / D_);
    float q = 0.f, q4 = 0.f;
#pragma unroll
    for (int i = 0; i < 4; ++i)
#pragma unroll
      for (int j = 0; j < 4; ++j) {
        float a = bf_lo(d[i][j]) - mean, b = bf_hi(d[i][j]) - mean;
        q += a * a + b * b;
      }
#pragma unroll
    for (int j = 0; j < 4; ++j) {
      float a = bf_lo(d[4][j]) - mean, b = bf_hi(d[4][j]) - mean;
      q4 += a * a + b * b;
    }
    q += q4 * lastw;
    q += __shfl_xor(q, 1); q += __shfl_xor(q, 2); q += __shfl_xor(q, 4); q += __shfl_xor(q, 8);
    const float var = q * (1.0f / D_);
    const float mb = rbf(mean);
    const float sd = rbf(sqrtf(rbf(rbf(var) + 1e-5f)));
    const float rinv = 1.0f / sd;
    if (c == 0) ((v2f*)(ws + WS_STATS))[row] = v2f{mb, rinv};
    char* xnt = ws + WS_XNT;
    const int tb = row >> 4, m = row & 15;
    const int cc = c & 3, T = cc >> 1, q0 = 2 * (cc & 1);
#pragma unroll
    for (int i = 0; i < 5; ++i) {
      u32 o[4];
#pragma unroll
      for (int j = 0; j < 4; ++j) {
        const u32 dd = pack_bf(bf_lo(d[i][j]) - mb, bf_hi(d[i][j]) - mb);
        o[j] = pack_bf(bf_lo(dd) * rinv, bf_hi(dd) * rinv);
      }
      const int kb = 4 * i + (c >> 2);
      char* base = xnt + ((size_t)(tb * 18 + kb) * 64 + (q0 * 16 + m)) * 16 + 8 * T;
      if (i < 4 || c < 8) {
        *(v2u*)(base) = v2u{o[0], o[1]};
        *(v2u*)(base + 256) = v2u{o[2], o[3]};
      }
    }
  } else {
    const int b = blockIdx.x - 128;
    const int tt = b / 9, ct = b % 9;
    const int tg = lane >> 5, cx = lane & 31;
    const int s0 = tt * 64 + w * 16 + tg * 8, d0 = ct * 64 + cx * 2;
    u32 kk[11], vv[8];
#pragma unroll
    for (int i = 0; i < 11; ++i) {
      const int s = s0 - 3 + i;
      const u32 t = *(const u32*)(kin + (size_t)(s < 0 ? 0 : s) * D_ + d0);
      kk[i] = (i < 3 && s < 0) ? 0u : t;
    }
#pragma unroll
    for (int i = 0; i < 8; ++i) vv[i] = *(const u32*)(vin + (size_t)(s0 + i) * D_ + d0);
    const v4u w0 = *(const v4u*)(shk + (size_t)d0 * 4);
    u16* kvt = (u16*)(ws + WS_KVT);
#pragma unroll
    for (int ch = 0; ch < 2; ++ch) {
      const u32 wa = w0[2 * ch], wb = w0[2 * ch + 1];
      const float t0 = bf_lo(wa), t1 = bf_hi(wa), t2 = bf_lo(wb), t3 = bf_hi(wb);
      float kf[11];
#pragma unroll
      for (int i = 0; i < 11; ++i) kf[i] = ch ? bf_hi(kk[i]) : bf_lo(kk[i]);
      float r[8];
#pragma unroll
      for (int i = 0; i < 8; i += 2) {
        u32 a = pack_bf(kf[i] * t3, kf[i + 1] * t3);
        u32 p = pack_bf(kf[i + 1] * t2, kf[i + 2] * t2);
        a = pack_bf(bf_lo(a) + bf_lo(p), bf_hi(a) + bf_hi(p));
        p = pack_bf(kf[i + 2] * t1, kf[i + 3] * t1);
        a = pack_bf(bf_lo(a) + bf_lo(p), bf_hi(a) + bf_hi(p));
        p = pack_bf(kf[i + 3] * t0, kf[i + 4] * t0);
        a = pack_bf(bf_lo(a) + bf_lo(p), bf_hi(a) + bf_hi(p));
        r[i] = bf_lo(a) * (ch ? bf_hi(vv[i]) : bf_lo(vv[i]));
        r[i + 1] = bf_hi(a) * (ch ? bf_hi(vv[i + 1]) : bf_lo(vv[i + 1]));
      }
      v4u o = {pack_bf(r[7], r[6]), pack_bf(r[5], r[4]), pack_bf(r[3], r[2]), pack_bf(r[1], r[0])};
      *(v4u*)(kvt + (size_t)(d0 + ch) * S_ + s0) = o;
    }
  }
  STAMP(ws, 0, 1);
}

constexpr int G_STAGE = 34816, G_NBUF = 4, G_LDS = G_STAGE * G_NBUF;
constexpr int G_XOFF = 18432;

typedef __attribute__((address_space(3))) v8s* lds_v8s_ptr;
typedef __attribute__((address_space(3))) char* lds_ptr;
DI v8s tr_frag(u32 p, int off_lo, int off_hi) {
  v4s a = __builtin_amdgcn_ds_read_tr16_b64_v4i16((lds_v4s_ptr)((lds_ptr)p + off_lo));
  v4s b = __builtin_amdgcn_ds_read_tr16_b64_v4i16((lds_v4s_ptr)((lds_ptr)p + off_hi));
  return __builtin_shufflevector(a, b, 0, 1, 2, 3, 4, 5, 6, 7);
}
DI v8s ld_frag(u32 p, int off) { return *(lds_v8s_ptr)((lds_ptr)p + off); }
DI u32 opaque(u32 v) { asm volatile("" : "+v"(v)); return v; }
DI u32 lds_addr(const void* p) { return (u32)(size_t)(lds_ptr)p; }
typedef __amdgpu_buffer_rsrc_t srd_t;
DI srd_t make_srd(const void* p, u32 bytes) {
  return __builtin_amdgcn_make_buffer_rsrc((void*)p, 0, bytes, 0x00020000);
}
DI void dma16(u32 m0v, u32 voff, srd_t srd, u32 soff) {
  asm volatile("s_mov_b32 m0, %0\n\ts_nop 0\n\tbuffer_load_dwordx4 %1, %2, %3 offen lds"
               :: "s"(m0v), "v"(voff), "s"(srd), "s"(soff) : "memory");
}
DI void dma16_sc1(u32 m0v, u32 voff, srd_t srd, u32 soff) {
  asm volatile("s_mov_b32 m0, %0\n\ts_nop 0\n\tbuffer_load_dwordx4 %1, %2, %3 offen sc1 lds"
               :: "s"(m0v), "v"(voff), "s"(srd), "s"(soff) : "memory");
}
DI void wait_count(u32* p, u32 target) {
  u32 spins = 0;
  while (__hip_atomic_load(p, __ATOMIC_RELAXED, __HIP_MEMORY_SCOPE_AGENT) < target) {
    __builtin_amdgcn_s_sleep(1);
    if (++spins > (1u << 24)) break;
  }
  __builtin_amdgcn_fence(__ATOMIC_ACQUIRE, "wavefront");
}
#define SCHED0() __builtin_amdgcn_sched_barrier(0)
DI void mfma16(v4f& c, const v8s& a, const v8s& b) {
  asm volatile("v_mfma_f32_16x16x32_bf16 %0, %1, %2, %0" : "+v"(c) : "v"(a), "v"(b));
}
DI void mfma16_first(v4f& c, const v8s& a, const v8s& b) {
  asm volatile("v_mfma_f32_16x16x32_bf16 %0, %1, %2, 0" : "=&v"(c) : "v"(a), "v"(b));
}
DI void mfma_drain() { asm volatile("s_nop 15\n\ts_nop 15" ::: "memory"); }
template <int N> DI void wait_vm() { asm volatile("s_waitcnt vmcnt(%0)" :: "n"(N) : "memory"); }
DI void stage_barrier() { asm volatile("s_waitcnt lgkmcnt(0)\n\ts_barrier" ::: "memory"); }

constexpr int C_KBYTES = 2080 * 8, C_XBYTES = 8 * 768, C_WAVE = C_KBYTES + C_XBYTES;

DI void conv_channel(char* kl, int ch, int lane, const u16* __restrict__ ssmk, const u16* __restrict__ ssmD,
                     char* __restrict__ ws) {
  char* xl = kl + C_KBYTES;
  const srd_t sk = make_srd(ssmk + (size_t)ch * S_, S_ * 2);
  const v4u* xs = (const v4u*)(ws + WS_KVT + (size_t)ch * S_ * 2);
  v4u xd[4];
#pragma unroll
  for (int it = 0; it < 4; ++it) xd[it] = xs[it * 64 + lane];
  u32 d0[17], d1[17], d2[17];
#pragma unroll
  for (int it = 0; it < 17; ++it) {
    const int dw = it * 64 + lane - 16;
    if (it == 0 || it == 16) {
      d0[it] = __builtin_amdgcn_raw_buffer_load_b32(sk, (u32)(dw * 4), 0, 0);
      d1[it] = __builtin_amdgcn_raw_buffer_load_b32(sk, (u32)(dw * 4 + 4), 0, 0);
      d2[it] = __builtin_amdgcn_raw_buffer_load_b32(sk, (u32)(dw * 4 + 8), 0, 0);
    } else {
      typedef u32 v3u __attribute__((ext_vector_type(3)));
      const v3u t = __builtin_amdgcn_raw_buffer_load_b96(sk, (u32)(dw * 4), 0, 0);
      d0[it] = t[0]; d1[it] = t[1]; d2[it] = t[2];
    }
  }
#pragma unroll
  for (int it = 0; it < 2; ++it) {
    const int z = it * 64 + lane;
    *(v4u*)(xl + (z >> 4) * 768 + (z & 15) * 16) = v4u{0u, 0u, 0u, 0u};
  }
#pragma unroll
  for (int it = 0; it < 4; ++it) {
    const int cidx = it * 64 + lane;
    *(v4u*)(xl + (cidx & 7) * 768 + ((cidx >> 3) + 16) * 16) = xd[it];
  }
#pragma unroll
  for (int it = 0; it < 17; ++it) {
    const int L = it * 64 + lane;
    v4u o = {d0[it], d1[it], __builtin_amdgcn_alignbit(d1[it], d0[it], 16), __builtin_amdgcn_alignbit(d2[it], d1[it], 16)};
    if (it < 16 || lane < 16) *(v4u*)(kl + L * 16) = o;
  }
  __builtin_amdgcn_s_waitcnt(0xC07F);
  __builtin_amdgcn_wave_barrier();

  const int i16 = lane & 15, q = lane >> 4;
  const u32 ap = opaque(lds_addr(kl) + (i16 - 8 * q - 7 + 32) * 8);
  const u32 bp = opaque(lds_addr(xl) + q * 768 + i16 * 16);
  v4f acc[4][2];
  v8s af[3][4], bfr[2][4];
  auto rdA = [&](int c, int j) -> v8s {
    const int delta = 64 * c + 16 * j;
    v4s lo = *(volatile __attribute__((address_space(3))) v4s*)((lds_ptr)ap + 8 * delta);
    v4s hi = *(volatile __attribute__((address_space(3))) v4s*)((lds_ptr)ap + 8 * delta + 32);
    return __builtin_shufflevector(lo, hi, 0, 1, 2, 3, 4, 5, 6, 7);
  };
  auto rdB = [&](int c, int n, int kb) -> v8s { return ld_frag(bp, 4 * kb * 768 + 16 * (16 * n - c + 16)); };
#pragma unroll
  for (int j = 0; j < 4; ++j) af[0][j] = rdA(0, j);
#pragma unroll
  for (int j = 0; j < 4; ++j) bfr[0][j] = rdB(0, j >> 1, j & 1);
  SCHED0();
  static_for<32>([&](auto CC) {
    constexpr int c = decltype(CC)::value;
    if constexpr (c + 1 < 32) {
#pragma unroll
      for (int j = 0; j < 4; ++j) af[(c + 1) % 3][j] = rdA(c + 1, j);
#pragma unroll
      for (int j = 0; j < 4; ++j)
        if (j >= 2 || c + 1 < 16) bfr[(c + 1) & 1][j] = rdB(c + 1, j >> 1, j & 1);
    }
    SCHED0();
#pragma unroll
    for (int n = 1; n >= 0; --n) {
      if (n == 0 && c >= 16) continue;
#pragma unroll
      for (int kb = 0; kb < 2; ++kb) {
#pragma unroll
        for (int ib = 0; ib < 4; ++ib) {
          const int j = ib - 2 * kb;
          if (c == 0 && j < 0) continue;
          const v8s& A = (j >= 0) ? af[c % 3][j >= 0 ? j : 0] : af[(c + 2) % 3][j < 0 ? j + 4 : 0];
          if (c == 0 && kb == 0) mfma16_first(acc[ib][n], A, bfr[c & 1][2 * n + kb]);
          else mfma16(acc[ib][n], A, bfr[c & 1][2 * n + kb]);
        }
      }
    }
    SCHED0();
  });
  mfma_drain();
  SCHED0();
  const float Dd = bf_lo((u32)ssmD[ch]);
  const srd_t sy = make_srd(ws + WS_YT + (size_t)ch * S_ * 2, S_ * 2);
#pragma unroll
  for (int n = 0; n < 2; ++n)
#pragma unroll
    for (int ib = 0; ib < 4; ++ib) {
      const int beta = 16 * n + i16;
      const v2u kvp = *(const v2u*)(xl + (2 * ib + (q >> 1)) * 768 + (beta + 16) * 16 + 8 * (1 - (q & 1)));
      const float k3 = bf_lo(kvp[0]), k2 = bf_hi(kvp[0]), k1 = bf_lo(kvp[1]), k0 = bf_hi(kvp[1]);
      const float y0 = rbf(acc[ib][n][0]) + rbf(k0 * Dd);
      const float y1 = rbf(acc[ib][n][1]) + rbf(k1 * Dd);
      const float y2 = rbf(acc[ib][n][2]) + rbf(k2 * Dd);
      const float y3 = rbf(acc[ib][n][3]) + rbf(k3 * Dd);
      __builtin_amdgcn_raw_buffer_store_b64(v2u{pack_bf(y0, y1), pack_bf(y2, y3)}, sy, (u32)((64 * beta + 16 * ib + 4 * q) * 2), 0, 16);
    }
  asm volatile("s_waitcnt vmcnt(0) lgkmcnt(0)" ::: "memory");
  if (lane == 0) __hip_atomic_fetch_add((u32*)(ws + WS_YCNT), 1u, __ATOMIC_RELAXED, __HIP_MEMORY_SCOPE_AGENT);
  __builtin_amdgcn_wave_barrier();
}

constexpr int H_LDS = 163840;
constexpr int A_STAGE = 32768;
constexpr int b_slot(int s) { return (s % 3) == 0 ? 65536 : (s % 3) == 1 ? 114688 : 0; }
constexpr int g_slot(int s) { return (s & 3) == 0 ? 49152 : (s & 3) == 1 ? 83968 : (s & 3) == 2 ? 118784 : 0; }

DI void h_signal(u32* p, int lane) {
  if (lane == 0) __hip_atomic_store(p, 1u, __ATOMIC_RELAXED, __HIP_MEMORY_SCOPE_AGENT);
}
DI void wait_flags(const u32* p) {
  u32 spins = 0;
  while (!__all(__hip_atomic_load(p, __ATOMIC_RELAXED, __HIP_MEMORY_SCOPE_AGENT) != 0u)) {
    __builtin_amdgcn_s_sleep(1);
    if (++spins > (1u << 24)) break;
  }
  __builtin_amdgcn_fence(__ATOMIC_ACQUIRE, "wavefront");
}
DI v4u h_pack(const v4f& g0v, const v4f& u0v, const v4f& g1v, const v4f& u1v) {
  v4u hv;
  {
    const u32 g0 = pack_bf(g0v[0], g0v[1]), g1 = pack_bf(g0v[2], g0v[3]);
    const u32 u0 = pack_bf(u0v[0], u0v[1]), u1 = pack_bf(u0v[2], u0v[3]);
    hv[0] = pack_bf(bf_lo(g0) * bf_lo(u0), bf_hi(g0) * bf_hi(u0));
    hv[1] = pack_bf(bf_lo(g1) * bf_lo(u1), bf_hi(g1) * bf_hi(u1));
  }
  {
    const u32 g0 = pack_bf(g1v[0], g1v[1]), g1 = pack_bf(g1v[2], g1v[3]);
    const u32 u0 = pack_bf(u1v[0], u1v[1]), u1 = pack_bf(u1v[2], u1v[3]);
    hv[2] = pack_bf(bf_lo(g0) * bf_lo(u0), bf_hi(g0) * bf_hi(u0));
    hv[3] = pack_bf(bf_lo(g1) * bf_lo(u1), bf_hi(g1) * bf_hi(u1));
  }
  return hv;
}
DI void poll_issue(u32& v, const u32* p) {
  asm volatile("global_load_dword %0, %1, off sc1" : "=v"(v) : "v"(p) : "memory");
}
DI void poll_check(u32& v, u32* p, u32 target) {
  asm volatile("" : "+v"(v));
  if (__builtin_amdgcn_readfirstlane(v) < target) wait_count(p, target);
}

DI void flags_check(u32& v, const u32* p) {
  asm volatile("" : "+v"(v));
  if (!__all(v != 0u)) wait_flags(p);
}

struct G2Dma {
  srd_t sw, sx;
  u32 wsoffA, wsoffB, xsoff, m0w, m0x, woff, soff, xoff;
};
DI void g2_piece(const G2Dma& d, int lane, int st, int i) {
  const u32 bo = g_slot(st);
  const int kb = st < 6 ? 6 * st : st < 12 ? 6 * (st - 6) + 1 : 6 * (st - 12) + 4;
  const int kb2 = kb + (st < 12 ? 2 : 1);
  const u32 wo = (st < 12 ? d.wsoffA : d.wsoffB) + (u32)kb * (32 * D_ * 2);
  if (i == 0) dma16(d.m0w + bo, d.woff, d.sw, wo);
  else if (i == 1) dma16(d.m0w + bo + 1024, d.woff, d.sw, wo + 128);
  else if (i == 2) { if (lane < 16) dma16(d.m0w + bo + 2048, d.soff, d.sw, wo + 256); }
  else if (i == 3) dma16_sc1(d.m0x + bo, d.xoff, d.sx, d.xsoff + kb * 1024);
  else dma16_sc1(d.m0x + bo + 1024, d.xoff, d.sx, d.xsoff + kb2 * 1024);
}

DI void gemm1_body(u32 sb, srd_t sw, srd_t sx, u32 wsoff, u32 xsoff, int w, int lane, int wm, int wn,
                   char* ws, int kb0, int tb0, u32* hsigA, const G2Dma& g2, const u32* hflg, v4u (&hB)[4]) {
  STAMP(ws, 1, 0);
  FS_DECL
  FSTAMP(0);
  const int g = lane >> 4, qq = (lane & 15) >> 2, p = lane & 3;
  const int rr = 4 * (g & 1) + qq;
  const int wl0 = rr * 128 + (2 * (rr >> 1)) * 16 + p * 8;
  const u32 woff = (u32)((lane >> 3) * (F_ * 2) + ((lane & 7) ^ (2 * (lane >> 4))) * 16);
  const u32 xoff = lane * 16;
  const srd_t sh = make_srd(ws + WS_HT, S_ * F_ * 2);
  v8s wf[3], xf[2][4];
  const u32 m0wB = sb + (w & 3) * 2048 + (w >> 2) * 1024;
  const u32 m0xB = sb + 16384 + w * 4096;
  auto dmaB = [&](int st, int i) {
    const u32 bo = b_slot(st);
    const u32 ko = (u32)st * (64 * F_ * 2);
    if (i < 2) dma16(m0wB + bo + i * 8192, woff, sw, wsoff + ko + 256 + i * (32 * F_ * 2));
    else dma16(m0xB + bo + (i - 2) * 1024, xoff, sx, xsoff + st * 2048 + ((i - 2) >> 1) * (18 * 1024) + ((i - 2) & 1) * 1024);
  };
  v4f accA[2][4][4];
  {
    constexpr int NU = 8;
    u32 wp[4], xp;
#pragma unroll
    for (int t = 0; t < 4; ++t) wp[t] = opaque((sb + wl0 + (g >> 1) * 4096 + wn * 1024) ^ (t << 5));
    xp = opaque(sb + 16384 + wm * 4096 + lane * 16);
    const u32 m0w = sb + (w & 3) * 4096 + (w >> 2) * 2048;
    const u32 m0x = sb + 16384 + w * 2048;
    auto dma_piece = [&](int st, int i) {
      const u32 bo = (st & 3) * A_STAGE;
      const u32 ko = (u32)st * (32 * F_ * 2);
      if (i < 2) dma16(m0w + bo + i * 1024, woff, sw, wsoff + ko + i * 128);
      else dma16(m0x + bo + (i - 2) * 1024, xoff, sx, xsoff + st * 1024 + (i - 2) * (18 * 1024));
    };
    auto rdW = [&](int u) -> v8s { return tr_frag(wp[u >> 1], (u & 1) * 2048, (u & 1) * 2048 + 2 * 4096); };
#pragma unroll
    for (int st = 0; st < 3; ++st)
#pragma unroll
      for (int i = 0; i < 4; ++i) dma_piece(st, i);
    wait_vm<8>();
    stage_barrier();
#pragma unroll
    for (int mb = 0; mb < 4; ++mb) xf[0][mb] = ld_frag(xp, mb * 1024);
    STAMP(ws, 1, 1);
    wf[0] = rdW(0);
    wf[1] = rdW(1);
    dma_piece(3, 0); dma_piece(3, 1);
    SCHED0();
    static_for<18 * NU>([&](auto UC) {
      constexpr int U = decltype(UC)::value, st = U / NU, u = U % NU;
      if constexpr (u == NU - 2 && st + 1 < 18) {
        if constexpr (st + 1 <= 15) wait_vm<8>(); else if constexpr (st + 1 == 16) wait_vm<4>(); else wait_vm<8>();
        stage_barrier();
        FSTAMP(1 + st);
        constexpr int dl = (((st + 1) & 3) == 0) ? -3 * A_STAGE : A_STAGE;
#pragma unroll
        for (int t = 0; t < 4; ++t) wp[t] = opaque(wp[t] + dl);
        xp = opaque(xp + dl);
#pragma unroll
        for (int mb = 0; mb < 4; ++mb) xf[(st + 1) & 1][mb] = ld_frag(xp, mb * 1024);
      }
      if constexpr (u + 2 < NU) wf[(U + 2) % 3] = rdW(u + 2);
      else if constexpr (st + 1 < 18) wf[(U + 2) % 3] = rdW(u + 2 - NU);
      if constexpr (u >= NU - 2) { if constexpr (st + 4 < 18) dma_piece(st + 4, u - (NU - 2)); }
      else if constexpr (u < 2) { if constexpr (st + 3 < 18) dma_piece(st + 3, u + 2); }
      constexpr int qB = U - (15 * NU + 6);
      if constexpr (qB >= 0 && qB < 12) dmaB(qB / 6, qB % 6);
      SCHED0();
      constexpr int T = u >> 1, gu = u & 1;
#pragma unroll
      for (int mb = 0; mb < 4; ++mb) {
        if constexpr (st == 0) mfma16_first(accA[gu][T][mb], wf[U % 3], xf[st & 1][mb]);
        else mfma16(accA[gu][T][mb], wf[U % 3], xf[st & 1][mb]);
      }
      SCHED0();
    });
  }
  STAMP(ws, 1, 2);
  FSTAMP(18);
  {
    constexpr int NU = 8;
    v4f acc[2][2][4];
    v4u hA[8];
    u32 wp[2], xp, pollA = 0;
#pragma unroll
    for (int t = 0; t < 2; ++t) wp[t] = opaque((sb + b_slot(0) + wl0 + (g >> 1) * 2048) ^ ((2 * wn + t) << 5));
    xp = opaque(sb + b_slot(0) + 16384 + wm * 8192 + lane * 16);
    auto rdW = [&](int u) -> v8s {
      return tr_frag(wp[(u >> 1) & 1], (u >> 2) * 8192 + (u & 1) * 1024, (u >> 2) * 8192 + (u & 1) * 1024 + 2 * 2048);
    };
    auto storeA = [&](int i) {
      __builtin_amdgcn_raw_buffer_store_b128(hA[i], sh, lane * 16, (u32)(((tb0 + (i >> 1)) * 144 + kb0 + 2 * wn + (i & 1)) * 1024), 16);
    };
    wait_vm<6>();
    stage_barrier();
#pragma unroll
    for (int mb = 0; mb < 4; ++mb) xf[0][mb] = ld_frag(xp, mb * 2048);
    wf[0] = rdW(0);
    wf[1] = rdW(1);
    dmaB(2, 0); dmaB(2, 1);
    mfma_drain();
    FSTAMP(19);
    STAMP(ws, 1, 3);
    SCHED0();
    static_for<9 * NU>([&](auto UC) {
      constexpr int U = decltype(UC)::value, st = U / NU, u = U % NU;
      if constexpr (u == 2) {
#pragma unroll
        for (int mb = 0; mb < 4; ++mb) xf[1][mb] = ld_frag(xp, mb * 2048 + 1024);
      }
      if constexpr (u == NU - 2 && st + 1 < 9) {
        constexpr int NW = st == 0 ? 8 : st == 1 ? 10 : st == 2 ? 9 : st == 3 ? 8 : st == 4 ? 9 : st == 5 ? 8 : st == 6 ? 8 : 0;
        wait_vm<NW>();
        stage_barrier();
        if constexpr (st == 3) h_signal(hsigA, lane);
        if constexpr (st == 5) poll_issue(pollA, hflg);
        if constexpr (st == 7) { h_signal(hsigA + 64 * 32, lane); flags_check(pollA, hflg); }
        FSTAMP(20 + st);
        constexpr int dl = b_slot(st + 1) - b_slot(st);
#pragma unroll
        for (int t = 0; t < 2; ++t) wp[t] = opaque(wp[t] + dl);
        xp = opaque(xp + dl);
#pragma unroll
        for (int mb = 0; mb < 4; ++mb) xf[0][mb] = ld_frag(xp, mb * 2048);
      }
      if constexpr (u + 2 < NU) wf[(U + 2) % 3] = rdW(u + 2);
      else if constexpr (st + 1 < 9) wf[(U + 2) % 3] = rdW(u + 2 - NU);
      if constexpr (u >= NU - 2) { if constexpr (st + 3 < 9) dmaB(st + 3, u - (NU - 2)); }
      else if constexpr (u < 4) { if constexpr (st + 2 < 9) dmaB(st + 2, u + 2); }
      if constexpr (st == 0 && u < 4) {
#pragma unroll
        for (int pr = 0; pr < 2; ++pr) {
          hA[u * 2 + pr] = h_pack(accA[0][2 * pr][u], accA[1][2 * pr][u], accA[0][2 * pr + 1][u], accA[1][2 * pr + 1][u]);
          asm volatile("" : "+v"(hA[u * 2 + pr]));
        }
      }
      if constexpr (st < 2 && (u == 4 || u == 5)) storeA(2 * (2 * st + u - 4));
      if constexpr (st >= 2 && st < 6 && u == 4) storeA(2 * (st - 2) + 1);
      constexpr int qG = U - (7 * NU + 6);
      if constexpr (qG >= 0 && qG < 4) { g2_piece(g2, lane, (2 * qG) / 3, (2 * qG) % 3); g2_piece(g2, lane, (2 * qG + 1) / 3, (2 * qG + 1) % 3); }
      if constexpr (qG == 4) g2_piece(g2, lane, 2, 2);
      if constexpr (qG >= 5 && qG < 8) { g2_piece(g2, lane, qG - 5, 3); g2_piece(g2, lane, qG - 5, 4); }
      SCHED0();
      constexpr int T = (u >> 1) & 1, gu = u & 1;
#pragma unroll
      for (int mb = 0; mb < 4; ++mb) {
        if constexpr (st == 0 && u < 4) mfma16_first(acc[gu][T][mb], wf[U % 3], xf[u >> 2][mb]);
        else mfma16(acc[gu][T][mb], wf[U % 3], xf[u >> 2][mb]);
      }
      SCHED0();
    });
    mfma_drain();
    SCHED0();
    STAMP(ws, 1, 4);
    FSTAMP(28);
    FS_DUMP(ws, 29);
#pragma unroll
    for (int mb = 0; mb < 4; ++mb) hB[mb] = h_pack(acc[0][0][mb], acc[1][0][mb], acc[0][1][mb], acc[1][1][mb]);
  }
}

template <int NT, bool PRE>
DI void gemm2_body(char* smem, u32 sb, const G2Dma& g2, int w, int lane, int kg, int wm, int tbase, int tile, int ksl,
                   const u16* __restrict__ xin, u16* __restrict__ out, char* ws, u32* hsigB, const v4u (&hB)[4],
                   u32 hBoff) {
  constexpr int NU = NT;
  constexpr int NO = (2 * NT + 3) / 4;
  v4f acc[NT][4];
  STAMP(ws, 2, 0);
  const int g = lane >> 4, qq = (lane & 15) >> 2, p = lane & 3;
  const int rr = 4 * (g & 1) + qq;
  const int wl0 = rr * 128 + (2 * (rr >> 1)) * 16 + p * 8;
  u32 wp[NT], xp;
#pragma unroll
  for (int t = 0; t < 4; ++t)
    wp[t] = opaque((sb + g_slot(0) + wl0 + ((g >> 1) + 4 * kg) * 2304 + (NT == 5 ? 1024 : 0)) ^ (t << 5));
  if constexpr (NT == 5) wp[4] = opaque(sb + g_slot(0) + 2048 + rr * 32 + p * 8 + ((g >> 1) + 4 * kg) * 2304);
  xp = opaque(sb + g_slot(0) + G_XOFF + wm * 8192 + kg * 1024 + lane * 16);
  auto rdW = [&](int u) -> v8s { return tr_frag(wp[u], 0, 2 * 2304); };
  const u32* hflg = (const u32*)(ws + WS_HCNT) + ((tile >> 2) * 4 + ksl) * 32 + (lane < 24 ? lane : 0);
  u32* ycnt = (u32*)(ws + WS_YCNT);
  v8s wf[3], xf[2][4];
  u32 pollB = 0, pollY = 0;
  if constexpr (!PRE) {
#pragma unroll
    for (int st = 0; st < 3; ++st)
#pragma unroll
      for (int i = 0; i < 3; ++i) g2_piece(g2, lane, st, i);
    wait_flags(hflg);
#pragma unroll
    for (int st = 0; st < 3; ++st) { g2_piece(g2, lane, st, 3); g2_piece(g2, lane, st, 4); }
  }
  wait_vm<4>();
  stage_barrier();
#pragma unroll
  for (int mb = 0; mb < 4; ++mb) xf[0][mb] = ld_frag(xp, mb * 2048);
  STAMP(ws, 2, 1);
  wf[0] = rdW(0);
  wf[1] = rdW(1);
  g2_piece(g2, lane, 3, 0); g2_piece(g2, lane, 3, 1);
  SCHED0();
  static_for<18 * NU>([&](auto UC) {
    constexpr int U = decltype(UC)::value, st = U / NU, u = U % NU;
    constexpr int SB = PRE ? 2 : 0;
    if constexpr (u == NU - 2 && st + 1 < 18) {
      constexpr int NW = st == 0 ? 7 + SB : st == 1 ? 11 + 2 * SB : st == 2 ? 10 + SB : st == 3 ? 10 + SB
                         : st + 1 <= 15 ? 10 : st + 1 == 16 ? 5 : 0;
      wait_vm<NW>();
      stage_barrier();
      if constexpr (st == 0) poll_issue(pollB, hflg + 64 * 32);
      if constexpr (st == 2) flags_check(pollB, hflg + 64 * 32);
      if constexpr (PRE && st == 4) h_signal(hsigB, lane);
      if constexpr (st == 6) poll_issue(pollB, hflg + 128 * 32);
      if constexpr (st == 8) flags_check(pollB, hflg + 128 * 32);
      if constexpr (st == 12) poll_issue(pollY, ycnt);
      constexpr int dl = g_slot(st + 1) - g_slot(st);
#pragma unroll
      for (int t = 0; t < NT; ++t) wp[t] = opaque(wp[t] + dl);
      xp = opaque(xp + dl);
#pragma unroll
      for (int mb = 0; mb < 4; ++mb) xf[(st + 1) & 1][mb] = ld_frag(xp, mb * 2048);
    }
    if constexpr (u + 2 < NU) wf[(U + 2) % 3] = rdW(u + 2);
    else if constexpr (st + 1 < 18) wf[(U + 2) % 3] = rdW(u + 2 - NU);
    if constexpr (u >= NU - 2) { if constexpr (st + 4 < 18) g2_piece(g2, lane, st + 4, u - (NU - 2)); }
    else if constexpr (u == 0) { if constexpr (st + 3 < 18) { g2_piece(g2, lane, st + 3, 2); g2_piece(g2, lane, st + 3, 3); } }
    else if constexpr (u == 1) { if constexpr (st + 3 < 18) g2_piece(g2, lane, st + 3, 4); }
    if constexpr (PRE && u == 1 && st < 2) {
#pragma unroll
      for (int mb = 2 * st; mb < 2 * st + 2; ++mb)
        __builtin_amdgcn_raw_buffer_store_b128(hB[mb], g2.sx, lane * 16, hBoff + mb * (144 * 1024), 16);
    }
    SCHED0();
#pragma unroll
    for (int mb = 0; mb < 4; ++mb) {
      if constexpr (st == 0) mfma16_first(acc[u][mb], wf[U % 3], xf[st & 1][mb]);
      else mfma16(acc[u][mb], wf[U % 3], xf[st & 1][mb]);
    }
    SCHED0();
  });
  STAMP(ws, 2, 2);
  poll_check(pollY, ycnt, 576u);
  const int j = 2 * wm + kg;
  const int tok0 = (tile >> 2) * 128 + j * 32 + (lane & 15) + 16 * (ksl & 1);
  const int dcol = (tile & 3) * 144 + tbase * 16 + (lane >> 4) * 4 + 16 * (ksl >> 1);
  v2u xv[NO]; u32 yv[NO][4]; v2f stt;
  {
    const srd_t sy = make_srd(ws + WS_YT, S_ * D_ * 2);
    stt = ((const v2f*)(ws + WS_STATS))[tok0];
#pragma unroll
    for (int i = 0; i < NO; ++i) {
      const int dc = (4 * i + ksl < 2 * NT) ? dcol + 32 * i : dcol;
      xv[i] = *(const v2u*)(xin + (size_t)tok0 * D_ + dc);
#pragma unroll
      for (int r = 0; r < 4; ++r) yv[i][r] = __builtin_amdgcn_raw_buffer_load_b16(sy, (u32)(((dc + r) * S_ + tok0) * 2), 0, 16);
    }
  }
  mfma_drain();
  SCHED0();
  stage_barrier();
  {
    char* wr = smem + ((wm * 2 + kg) * 9 + tbase) * 2048 + lane * 16;
#pragma unroll
    for (int T = 0; T < NT; ++T)
#pragma unroll
      for (int b2 = 0; b2 < 2; ++b2) *(v4f*)(wr + (T * 2 + b2) * 1024) = kg ? acc[T][b2] : acc[T][2 + b2];
  }
  stage_barrier();
  v4f r[2 * NT];
  {
    const char* rd = smem + ((wm * 2 + (kg ^ 1)) * 9 + tbase) * 2048 + lane * 16;
#pragma unroll
    for (int T = 0; T < NT; ++T)
#pragma unroll
      for (int b2 = 0; b2 < 2; ++b2) {
        const v4f o = *(const v4f*)(rd + (T * 2 + b2) * 1024);
        r[T * 2 + b2] = kg ? (o + acc[T][2 + b2]) : (acc[T][b2] + o);
      }
  }
  const srd_t sp = make_srd(ws + WS_PART, 64 * 4 * 8 * 4 * 3072);
#pragma unroll
  for (int f = 0; f < 2 * NT; ++f)
    if ((f & 3) != ksl)
      __builtin_amdgcn_raw_buffer_store_b128(__builtin_bit_cast(v4u, r[f]), sp, lane * 16,
                                             (u32)((((tile * 4 + (f & 3)) * 8 + w) * 4 + ksl) * 3072 + (f >> 2) * 1024), 16);
  v4f own[NO];
#pragma unroll
  for (int i = 0; i < NO; ++i) {
    const v4f a0 = r[4 * i], a1 = r[4 * i + 1];
    const v4f a2 = (4 * i + 2 < 2 * NT) ? r[(4 * i + 2 < 2 * NT) ? 4 * i + 2 : 0] : a0;
    const v4f a3 = (4 * i + 3 < 2 * NT) ? r[(4 * i + 3 < 2 * NT) ? 4 * i + 3 : 0] : a0;
    own[i] = ksl == 0 ? a0 : ksl == 1 ? a1 : ksl == 2 ? a2 : a3;
  }
  asm volatile("s_waitcnt vmcnt(0)" ::: "memory");
  u32* flags = (u32*)(ws + WS_CNT);
  if (lane < 4 && lane != ksl)
    __hip_atomic_store(flags + ((tile * 4 + lane) * 8 + w) * 16 + ksl, 1u, __ATOMIC_RELAXED, __HIP_MEMORY_SCOPE_AGENT);
  STAMP(ws, 2, 3);
  float xa[NO][4];
#pragma unroll
  for (int i = 0; i < NO; ++i)
#pragma unroll
    for (int e = 0; e < 4; ++e) {
      const float xf_ = (e & 1) ? bf_hi(xv[i][e >> 1]) : bf_lo(xv[i][e >> 1]);
      const float xn = rbf(rbf(xf_ - stt[0]) * stt[1]);
      xa[i][e] = rbf(xf_ + rbf(xn * bf_lo(yv[i][e])));
    }
  {
    u32* fl = flags + ((tile * 4 + ksl) * 8 + w) * 16;
    u32 spins = 0;
    for (;;) {
      const u32 v = (lane < 4 && lane != ksl) ? __hip_atomic_load(fl + lane, __ATOMIC_RELAXED, __HIP_MEMORY_SCOPE_AGENT) : 1u;
      if (__all(v != 0u)) break;
      __builtin_amdgcn_s_sleep(1);
      if (++spins > (1u << 24)) break;
    }
  }
  __builtin_amdgcn_fence(__ATOMIC_ACQUIRE, "wavefront");
  STAMP(ws, 2, 4);
  v4f pp[3][NO];
#pragma unroll
  for (int i3 = 0; i3 < 3; ++i3) {
    const int src = i3 + (i3 >= ksl ? 1 : 0);
    const u32 po = (u32)((((tile * 4 + ksl) * 8 + w) * 4 + src) * 3072);
#pragma unroll
    for (int i = 0; i < NO; ++i)
      pp[i3][i] = __builtin_bit_cast(v4f, __builtin_amdgcn_raw_buffer_load_b128(sp, lane * 16, po + i * 1024, 16));
  }
#pragma unroll
  for (int i = 0; i < NO; ++i) {
    const v4f s0 = ksl == 0 ? own[i] : pp[0][i];
    const v4f s1 = ksl == 1 ? own[i] : (ksl < 1 ? pp[0][i] : pp[1][i]);
    const v4f s2 = ksl == 2 ? own[i] : (ksl < 2 ? pp[1][i] : pp[2][i]);
    const v4f s3 = ksl == 3 ? own[i] : pp[2][i];
    const v4f B = (s0 + s1) + (s2 + s3);
    const float o0 = xa[i][0] + rbf(B[0]), o1 = xa[i][1] + rbf(B[1]);
    const float o2 = xa[i][2] + rbf(B[2]), o3 = xa[i][3] + rbf(B[3]);
    if (4 * i + ksl < 2 * NT)
      *(v2u*)(out + (size_t)tok0 * D_ + dcol + 32 * i) = v2u{pack_bf(o0, o1), pack_bf(o2, o3)};
  }
  STAMP(ws, 2, 5);
}

__global__ __launch_bounds__(512, 2) void fused_kernel(const u16* __restrict__ Wg, const u16* __restrict__ Wu,
                                                       const u16* __restrict__ Wd, const u16* __restrict__ ssmk,
                                                       const u16* __restrict__ ssmD, const u16* __restrict__ xin,
                                                       u16* __restrict__ out, char* __restrict__ ws) {
  extern __shared__ __attribute__((aligned(1024))) char smem[];
  const int bid = blockIdx.x;
  const int xcd = bid & 7, idx = bid >> 3;
  const int tid = threadIdx.x, lane = tid & 63;
  const int w = __builtin_amdgcn_readfirstlane(tid >> 6);
  const int ksl = xcd >> 1;
  const bool isconv = idx >= 24;
  const int tile2 = isconv ? (2 * (idx - 24) + (xcd & 1)) * 4 + 3 : (2 * (idx / 3) + (xcd & 1)) * 4 + idx % 3;
  const u32 sb = lds_addr(smem);
  G2Dma g2;
  g2.sw = make_srd(Wd, D_ * F_ * 2);
  g2.sx = make_srd(ws + WS_HT, S_ * F_ * 2);
  g2.wsoffA = (u32)((ksl * 1152 + 8 * (w & 3) + (w >> 2) * 64) * (D_ * 2) + (tile2 & 3) * 288);
  g2.wsoffB = (u32)((ksl * 1152 + 8 * w) * (D_ * 2) + (tile2 & 3) * 288);
  g2.xsoff = (u32)((((tile2 >> 2) * 8 + w) * 144 + ksl * 36) * 1024);
  g2.m0w = sb + w * 2304;
  g2.m0x = sb + G_XOFF + w * 2048;
  g2.woff = (u32)((lane >> 3) * (D_ * 2) + ((lane & 7) ^ (2 * (lane >> 4))) * 16);
  g2.soff = (u32)(((lane >> 1) & 7) * (D_ * 2) + (lane & 1) * 16);
  g2.xoff = lane * 16;
  const int kg2 = w >> 2, wm2 = (w >> 1) & 1, wn2 = (w & 1) ^ kg2;
  v4u hB[4];
  if (isconv) {
    const int cb = xcd * 8 + (idx - 24);
    STAMP(ws, 3, 0);
#pragma unroll 1
    for (int c = w; c < 9 && w < 7; c += 7) {
      conv_channel(smem + w * C_WAVE, cb * 9 + c, lane, ssmk, ssmD, ws);
      STAMP(ws, 3, 1 + c / 7);
    }
    stage_barrier();
#pragma unroll
    for (int i = 0; i < 4; ++i) hB[i] = v4u{0u, 0u, 0u, 0u};
    if (wn2 == 0) gemm2_body<5, false>(smem, sb, g2, w, lane, kg2, wm2, 4, tile2, ksl, xin, out, ws, nullptr, hB, 0u);
    else          gemm2_body<4, false>(smem, sb, g2, w, lane, kg2, wm2, 0, tile2, ksl, xin, out, ws, nullptr, hB, 0u);
  } else {
    const int tile_n = xcd * 3 + idx % 3, tile_m = idx / 3;
    const int wm = w & 3, wn = w >> 2;
    const srd_t sw = make_srd((w >> 2) ? Wu : Wg, D_ * F_ * 2);
    const srd_t sx = make_srd(ws + WS_XNT, S_ * D_ * 2);
    const u32 wsoff = (u32)((8 * (w & 3)) * (F_ * 2) + tile_n * 384);
    const u32 xsoff = (u32)((tile_m * 16 + 2 * w) * (18 * 1024));
    u32* hsigA = (u32*)(ws + WS_HCNT) + ((2 * tile_m + (wm >> 1)) * 4 + ksl) * 32 + (tile_n % 6) * 4 + (wm & 1) * 2 + wn;
    const u32* hflg = (const u32*)(ws + WS_HCNT) + ((tile2 >> 2) * 4 + ksl) * 32 + (lane < 24 ? lane : 0);
    gemm1_body(sb, sw, sx, wsoff, xsoff, w, lane, wm, wn, ws, tile_n * 6, tile_m * 16 + wm * 4, hsigA, g2, hflg, hB);
    const u32 hBoff = (u32)(((tile_m * 16 + wm * 4) * 144 + tile_n * 6 + 4 + wn) * 1024);
    if (wn2 == 0) gemm2_body<5, true>(smem, sb, g2, w, lane, kg2, wm2, 4, tile2, ksl, xin, out, ws, hsigA + 128 * 32, hB, hBoff);
    else          gemm2_body<4, true>(smem, sb, g2, w, lane, kg2, wm2, 0, tile2, ksl, xin, out, ws, hsigA + 128 * 32, hB, hBoff);
  }
}

extern "C" void kernel_launch(void* const* d_in, const int* in_sizes, int n_in, void* d_out, int out_size,
                              void* d_ws, size_t ws_size, hipStream_t stream) {
  const u16* q = (const u16*)d_in[0];
  const u16* k = (const u16*)d_in[1];
  const u16* v = (const u16*)d_in[2];
  const u16* shk = (const u16*)d_in[3];
  const u16* ssmk = (const u16*)d_in[4];
  const u16* ssmD = (const u16*)d_in[5];
  const u16* Wg = (const u16*)d_in[7];
  const u16* Wu = (const u16*)d_in[8];
  const u16* Wd = (const u16*)d_in[9];
  char* ws = (char*)d_ws;
  (void)hipFuncSetAttribute((const void*)fused_kernel, hipFuncAttributeMaxDynamicSharedMemorySize, H_LDS);
  pre_kernel<<<128 + 288, 256, 0, stream>>>(q, k, v, shk, ws);
  fused_kernel<<<256, 512, H_LDS, stream>>>(Wg, Wu, Wd, ssmk, ssmD, q, (u16*)d_out, ws);
}
```

```cpp
#include <hip/hip_runtime.h>
#include <hip/hip_bf16.h>
#include <stdint.h>
#include <utility>

typedef unsigned short u16;
typedef unsigned int u32;
typedef short v4s __attribute__((ext_vector_type(4)));
typedef short v8s __attribute__((ext_vector_type(8)));
typedef float v4f __attribute__((ext_vector_type(4)));
typedef float v2f __attribute__((ext_vector_type(2)));
typedef u32 v4u __attribute__((ext_vector_type(4)));
typedef u32 v2u __attribute__((ext_vector_type(2)));
typedef __bf16 v2bf __attribute__((ext_vector_type(2)));
typedef __attribute__((address_space(3))) v4s* lds_v4s_ptr;

constexpr int S_ = 2048, D_ = 576, F_ = 4608;

#define DI __device__ __forceinline__

template <int... I, class Fn>
DI void static_for_impl(std::integer_sequence<int, I...>, Fn&& f) { (f(std::integral_constant<int, I>{}), ...); }
template <int N, class Fn>
DI void static_for(Fn&& f) { static_for_impl(std::make_integer_sequence<int, N>{}, f); }

DI float bf_lo(u32 w) { return __uint_as_float(w << 16); }
DI float bf_hi(u32 w) { return __uint_as_float(w & 0xffff0000u); }
DI float rbf(float f) { return (float)(__bf16)f; }
DI u32 pack_bf(float lo, float hi) {
  v2f t = {lo, hi};
  v2bf b = __builtin_convertvector(t, v2bf);
  return __builtin_bit_cast(u32, b);
}

template <int CTRL> DI float dpp_f(float v) {
  return __builtin_bit_cast(float, __builtin_amdgcn_update_dpp(0, __builtin_bit_cast(int, v), CTRL, 0xf, 0xf, false));
}
DI float sum32(float v) {
  v += dpp_f<0x128>(v);
  v += dpp_f<0x124>(v);
  v += dpp_f<0x122>(v);
  v += dpp_f<0x121>(v);
  return v + __shfl_xor(v, 16);
}
#define STAMP(ws, kid, idx)
#define FS_DECL
#define FSTAMP(i)
#define FS_DUMP(ws, n)
#define FS_DUMP2(ws, n)
constexpr size_t WS_XNT = 0;
constexpr size_t WS_STATS = WS_XNT + (size_t)S_ * D_ * 2;
constexpr size_t WS_KVT = WS_STATS + (size_t)S_ * 8;
constexpr size_t WS_YT = WS_KVT + (size_t)S_ * D_ * 2;
constexpr size_t WS_HT = WS_YT + (size_t)S_ * D_ * 2;
constexpr size_t WS_PART = WS_HT + (size_t)S_ * F_ * 2;
constexpr size_t WS_CNT = WS_PART + (size_t)64 * 4 * 8 * 4 * 3072;
constexpr size_t WS_HCNT = WS_CNT + 2048 * 64;
constexpr size_t WS_YCNT = WS_HCNT + 3 * 64 * 128;

__global__ __launch_bounds__(256) void pre_kernel(const u16* __restrict__ x, const u16* __restrict__ kin,
                                                  const u16* __restrict__ vin, const u16* __restrict__ shk,
                                                  char* __restrict__ ws) {
  const int lane = threadIdx.x & 63, w = threadIdx.x >> 6;
  STAMP(ws, 0, 0);
  if (blockIdx.x < 8) *(v4u*)(ws + WS_CNT + (size_t)(blockIdx.x * 256 + threadIdx.x) * 64) = v4u{0u, 0u, 0u, 0u};
  else if (blockIdx.x < 15) *(v4u*)(ws + WS_HCNT + (size_t)((blockIdx.x - 8) * 256 + threadIdx.x) * 16) = v4u{0u, 0u, 0u, 0u};
  if (blockIdx.x < 256) {
    const int c = lane & 31;
    const int row = blockIdx.x * 8 + w * 2 + (lane >> 5);
    const v4u* src = (const v4u*)(x + (size_t)row * D_);
    v4u d[3];
    d[0] = src[c]; d[1] = src[c + 32]; d[2] = src[64 + (c & 7)];
    const float lastw = (c < 8) ? 1.f : 0.f;
    float f[3][8];
#pragma unroll
    for (int i = 0; i < 3; ++i)
#pragma unroll
      for (int j = 0; j < 4; ++j) { f[i][2 * j] = bf_lo(d[i][j]); f[i][2 * j + 1] = bf_hi(d[i][j]); }
    float s0 = 0.f, s1 = 0.f, s2 = 0.f;
#pragma unroll
    for (int j = 0; j < 8; ++j) { s0 += f[0][j]; s1 += f[1][j]; s2 += f[2][j]; }
    const float mean = sum32(s0 + s1 + s2 * lastw) * (1.0f / D_);
    float q0 = 0.f, q1 = 0.f, q2 = 0.f;
#pragma unroll
    for (int j = 0; j < 8; ++j) {
      const float a = f[0][j] - mean, b = f[1][j] - mean, e = f[2][j] - mean;
      q0 += a * a; q1 += b * b; q2 += e * e;
    }
    const float var = sum32(q0 + q1 + q2 * lastw) * (1.0f / D_);
    const float mb = rbf(mean);
    const float sd = rbf(sqrtf(rbf(rbf(var) + 1e-5f)));
    const float rinv = 1.0f / sd;
    if (c == 0) ((v2f*)(ws + WS_STATS))[row] = v2f{mb, rinv};
    char* xnt = ws + WS_XNT;
    const int tb = row >> 4, m = row & 15;
    const int cc = c & 3, T = cc >> 1, q0i = 2 * (cc & 1);
#pragma unroll
    for (int i = 0; i < 3; ++i) {
      u32 o[4];
#pragma unroll
      for (int j = 0; j < 4; ++j) {
        const u32 dd = pack_bf(f[i][2 * j] - mb, f[i][2 * j + 1] - mb);
        o[j] = pack_bf(bf_lo(dd) * rinv, bf_hi(dd) * rinv);
      }
      const int kb = 8 * i + (c >> 2);
      char* base = xnt + ((size_t)(tb * 18 + kb) * 64 + (q0i * 16 + m)) * 16 + 8 * T;
      if (i < 2 || c < 8) {
        *(v2u*)(base) = v2u{o[0], o[1]};
        *(v2u*)(base + 256) = v2u{o[2], o[3]};
      }
    }
  } else {
    const int b = blockIdx.x - 256;
    const int tt = b / 9, ct = b % 9;
    const int tg = lane >> 5, cx = lane & 31;
    const int s0 = tt * 64 + w * 16 + tg * 8, d0 = ct * 64 + cx * 2;
    u32 kk[11], vv[8];
#pragma unroll
    for (int i = 0; i < 11; ++i) {
      const int s = s0 - 3 + i;
      const u32 t = *(const u32*)(kin + (size_t)(s < 0 ? 0 : s) * D_ + d0);
      kk[i] = (i < 3 && s < 0) ? 0u : t;
    }
#pragma unroll
    for (int i = 0; i < 8; ++i) vv[i] = *(const u32*)(vin + (size_t)(s0 + i) * D_ + d0);
    const v4u w0 = *(const v4u*)(shk + (size_t)d0 * 4);
    u16* kvt = (u16*)(ws + WS_KVT);
#pragma unroll
    for (int ch = 0; ch < 2; ++ch) {
      const u32 wa = w0[2 * ch], wb = w0[2 * ch + 1];
      const float t0 = bf_lo(wa), t1 = bf_hi(wa), t2 = bf_lo(wb), t3 = bf_hi(wb);
      float kf[11];
#pragma unroll
      for (int i = 0; i < 11; ++i) kf[i] = ch ? bf_hi(kk[i]) : bf_lo(kk[i]);
      float r[8];
#pragma unroll
      for (int i = 0; i < 8; i += 2) {
        u32 a = pack_bf(kf[i] * t3, kf[i + 1] * t3);
        u32 p = pack_bf(kf[i + 1] * t2, kf[i + 2] * t2);
        a = pack_bf(bf_lo(a) + bf_lo(p), bf_hi(a) + bf_hi(p));
        p = pack_bf(kf[i + 2] * t1, kf[i + 3] * t1);
        a = pack_bf(bf_lo(a) + bf_lo(p), bf_hi(a) + bf_hi(p));
        p = pack_bf(kf[i + 3] * t0, kf[i + 4] * t0);
        a = pack_bf(bf_lo(a) + bf_lo(p), bf_hi(a) + bf_hi(p));
        r[i] = bf_lo(a) * (ch ? bf_hi(vv[i]) : bf_lo(vv[i]));
        r[i + 1] = bf_hi(a) * (ch ? bf_hi(vv[i + 1]) : bf_lo(vv[i + 1]));
      }
      v4u o = {pack_bf(r[7], r[6]), pack_bf(r[5], r[4]), pack_bf(r[3], r[2]), pack_bf(r[1], r[0])};
      *(v4u*)(kvt + (size_t)(d0 + ch) * S_ + s0) = o;
    }
  }
  STAMP(ws, 0, 1);
}

constexpr int G_STAGE = 34816, G_NBUF = 4, G_LDS = G_STAGE * G_NBUF;
constexpr int G_XOFF = 18432;

typedef __attribute__((address_space(3))) v8s* lds_v8s_ptr;
typedef __attribute__((address_space(3))) char* lds_ptr;
DI v8s tr_frag(u32 p, int off_lo, int off_hi) {
  v4s a = __builtin_amdgcn_ds_read_tr16_b64_v4i16((lds_v4s_ptr)((lds_ptr)p + off_lo));
  v4s b = __builtin_amdgcn_ds_read_tr16_b64_v4i16((lds_v4s_ptr)((lds_ptr)p + off_hi));
  return __builtin_shufflevector(a, b, 0, 1, 2, 3, 4, 5, 6, 7);
}
DI v4s tr_half(u32 p, int off) { return __builtin_amdgcn_ds_read_tr16_b64_v4i16((lds_v4s_ptr)((lds_ptr)p + off)); }
DI v8s join(const v4s& a, const v4s& b) { return __builtin_shufflevector(a, b, 0, 1, 2, 3, 4, 5, 6, 7); }
DI v8s ld_frag(u32 p, int off) { return *(lds_v8s_ptr)((lds_ptr)p + off); }
DI u32 opaque(u32 v) { asm volatile("" : "+v"(v)); return v; }
DI u32 lds_addr(const void* p) { return (u32)(size_t)(lds_ptr)p; }
typedef __amdgpu_buffer_rsrc_t srd_t;
DI srd_t make_srd(const void* p, u32 bytes) {
  return __builtin_amdgcn_make_buffer_rsrc((void*)p, 0, bytes, 0x00020000);
}
DI void dma16(u32 m0v, u32 voff, srd_t srd, u32 soff) {
  asm volatile("s_mov_b32 m0, %0\n\ts_nop 0\n\tbuffer_load_dwordx4 %1, %2, %3 offen lds"
               :: "s"(m0v), "v"(voff), "s"(srd), "s"(soff) : "memory");
}
DI void dma16_sc1(u32 m0v, u32 voff, srd_t srd, u32 soff) {
  asm volatile("s_mov_b32 m0, %0\n\ts_nop 0\n\tbuffer_load_dwordx4 %1, %2, %3 offen sc1 lds"
               :: "s"(m0v), "v"(voff), "s"(srd), "s"(soff) : "memory");
}
DI void wait_count(u32* p, u32 target) {
  u32 spins = 0;
  while (__hip_atomic_load(p, __ATOMIC_RELAXED, __HIP_MEMORY_SCOPE_AGENT) < target) {
    __builtin_amdgcn_s_sleep(1);
    if (++spins > (1u << 24)) break;
  }
  __builtin_amdgcn_fence(__ATOMIC_ACQUIRE, "wavefront");
}
#define SCHED0() __builtin_amdgcn_sched_barrier(0)
DI void mfma16(v4f& c, const v8s& a, const v8s& b) {
  asm volatile("v_mfma_f32_16x16x32_bf16 %0, %1, %2, %0" : "+v"(c) : "v"(a), "v"(b));
}
DI void mfma16_first(v4f& c, const v8s& a, const v8s& b) {
  asm volatile("v_mfma_f32_16x16x32_bf16 %0, %1, %2, 0" : "=&v"(c) : "v"(a), "v"(b));
}
DI void mfma_drain() { asm volatile("s_nop 15\n\ts_nop 15" ::: "memory"); }
template <int N> DI void wait_vm() { asm volatile("s_waitcnt vmcnt(%0)" :: "n"(N) : "memory"); }
DI void stage_barrier() { asm volatile("s_waitcnt lgkmcnt(0)\n\ts_barrier" ::: "memory"); }

constexpr int C_KBYTES = 2080 * 8, C_XBYTES = 8 * 768, C_WAVE = C_KBYTES + C_XBYTES;

DI void conv_channel(char* kl, int ch, int lane, const u16* __restrict__ ssmk, const u16* __restrict__ ssmD,
                     char* __restrict__ ws) {
  char* xl = kl + C_KBYTES;
  const srd_t sk = make_srd(ssmk + (size_t)ch * S_, S_ * 2);
  const v4u* xs = (const v4u*)(ws + WS_KVT + (size_t)ch * S_ * 2);
  v4u xd[4];
#pragma unroll
  for (int it = 0; it < 4; ++it) xd[it] = xs[it * 64 + lane];
  u32 d0[17], d1[17], d2[17];
#pragma unroll
  for (int it = 0; it < 17; ++it) {
    const int dw = it * 64 + lane - 16;
    if (it == 0 || it == 16) {
      d0[it] = __builtin_amdgcn_raw_buffer_load_b32(sk, (u32)(dw * 4), 0, 0);
      d1[it] = __builtin_amdgcn_raw_buffer_load_b32(sk, (u32)(dw * 4 + 4), 0, 0);
      d2[it] = __builtin_amdgcn_raw_buffer_load_b32(sk, (u32)(dw * 4 + 8), 0, 0);
    } else {
      typedef u32 v3u __attribute__((ext_vector_type(3)));
      const v3u t = __builtin_amdgcn_raw_buffer_load_b96(sk, (u32)(dw * 4), 0, 0);
      d0[it] = t[0]; d1[it] = t[1]; d2[it] = t[2];
    }
  }
#pragma unroll
  for (int it = 0; it < 2; ++it) {
    const int z = it * 64 + lane;
    *(v4u*)(xl + (z >> 4) * 768 + (z & 15) * 16) = v4u{0u, 0u, 0u, 0u};
  }
#pragma unroll
  for (int it = 0; it < 4; ++it) {
    const int cidx = it * 64 + lane;
    *(v4u*)(xl + (cidx & 7) * 768 + ((cidx >> 3) + 16) * 16) = xd[it];
  }
#pragma unroll
  for (int it = 0; it < 17; ++it) {
    const int L = it * 64 + lane;
    v4u o = {d0[it], d1[it], __builtin_amdgcn_alignbit(d1[it], d0[it], 16), __builtin_amdgcn_alignbit(d2[it], d1[it], 16)};
    if (it < 16 || lane < 16) *(v4u*)(kl + L * 16) = o;
  }
  __builtin_amdgcn_s_waitcnt(0xC07F);
  __builtin_amdgcn_wave_barrier();

  const int i16 = lane & 15, q = lane >> 4;
  const u32 ap = opaque(lds_addr(kl) + (i16 - 8 * q - 7 + 32) * 8);
  const u32 bp = opaque(lds_addr(xl) + q * 768 + i16 * 16);
  v4f acc[4][2];
  v8s af[3][4], bfr[2][4];
  auto rdA = [&](int c, int j) -> v8s {
    const int delta = 64 * c + 16 * j;
    v4s lo = *(volatile __attribute__((address_space(3))) v4s*)((lds_ptr)ap + 8 * delta);
    v4s hi = *(volatile __attribute__((address_space(3))) v4s*)((lds_ptr)ap + 8 * delta + 32);
    return __builtin_shufflevector(lo, hi, 0, 1, 2, 3, 4, 5, 6, 7);
  };
  auto rdB = [&](int c, int n, int kb) -> v8s { return ld_frag(bp, 4 * kb * 768 + 16 * (16 * n - c + 16)); };
#pragma unroll
  for (int j = 0; j < 4; ++j) af[0][j] = rdA(0, j);
#pragma unroll
  for (int j = 0; j < 4; ++j) bfr[0][j] = rdB(0, j >> 1, j & 1);
  SCHED0();
  static_for<32>([&](auto CC) {
    constexpr int c = decltype(CC)::value;
    if constexpr (c + 1 < 32) {
#pragma unroll
      for (int j = 0; j < 4; ++j) af[(c + 1) % 3][j] = rdA(c + 1, j);
#pragma unroll
      for (int j = 0; j < 4; ++j)
        if (j >= 2 || c + 1 < 16) bfr[(c + 1) & 1][j] = rdB(c + 1, j >> 1, j & 1);
    }
    SCHED0();
#pragma unroll
    for (int n = 1; n >= 0; --n) {
      if (n == 0 && c >= 16) continue;
#pragma unroll
      for (int kb = 0; kb < 2; ++kb) {
#pragma unroll
        for (int ib = 0; ib < 4; ++ib) {
          const int j = ib - 2 * kb;
          if (c == 0 && j < 0) continue;
          const v8s& A = (j >= 0) ? af[c % 3][j >= 0 ? j : 0] : af[(c + 2) % 3][j < 0 ? j + 4 : 0];
          if (c == 0 && kb == 0) mfma16_first(acc[ib][n], A, bfr[c & 1][2 * n + kb]);
          else mfma16(acc[ib][n], A, bfr[c & 1][2 * n + kb]);
        }
      }
    }
    SCHED0();
  });
  mfma_drain();
  SCHED0();
  const float Dd = bf_lo((u32)ssmD[ch]);
  const srd_t sy = make_srd(ws + WS_YT + (size_t)ch * S_ * 2, S_ * 2);
#pragma unroll
  for (int n = 0; n < 2; ++n)
#pragma unroll
    for (int ib = 0; ib < 4; ++ib) {
      const int beta = 16 * n + i16;
      const v2u kvp = *(const v2u*)(xl + (2 * ib + (q >> 1)) * 768 + (beta + 16) * 16 + 8 * (1 - (q & 1)));
      const float k3 = bf_lo(kvp[0]), k2 = bf_hi(kvp[0]), k1 = bf_lo(kvp[1]), k0 = bf_hi(kvp[1]);
      const float y0 = rbf(acc[ib][n][0]) + rbf(k0 * Dd);
      const float y1 = rbf(acc[ib][n][1]) + rbf(k1 * Dd);
      const float y2 = rbf(acc[ib][n][2]) + rbf(k2 * Dd);
      const float y3 = rbf(acc[ib][n][3]) + rbf(k3 * Dd);
      __builtin_amdgcn_raw_buffer_store_b64(v2u{pack_bf(y0, y1), pack_bf(y2, y3)}, sy, (u32)((64 * beta + 16 * ib + 4 * q) * 2), 0, 16);
    }
  asm volatile("s_waitcnt vmcnt(0) lgkmcnt(0)" ::: "memory");
  if (lane == 0) __hip_atomic_fetch_add((u32*)(ws + WS_YCNT), 1u, __ATOMIC_RELAXED, __HIP_MEMORY_SCOPE_AGENT);
  __builtin_amdgcn_wave_barrier();
}

constexpr int H_LDS = 163840;
constexpr int A_STAGE = 32768;
constexpr int b_slot(int s) { return (s % 3) == 0 ? 65536 : (s % 3) == 1 ? 114688 : 0; }
constexpr int g_slot(int s) { return (s & 3) == 0 ? 49152 : (s & 3) == 1 ? 83968 : (s & 3) == 2 ? 118784 : 0; }
constexpr int gb_store(int st) { return st == 1 ? 2 : st == 2 ? 2 : (st >= 3 && st <= 6) ? 1 : 0; }
constexpr int gb_extra(int st) { return (st == 4 || st == 6) ? 1 : 0; }
constexpr int gb_wait(int st) {
  return st >= 7 ? 0 : (st > 0 ? gb_store(st - 1) + gb_extra(st - 1) : 0) + 6 + gb_store(st);
}

DI void h_signal(u32* p, int lane) {
  if (lane == 0) __hip_atomic_store(p, 1u, __ATOMIC_RELAXED, __HIP_MEMORY_SCOPE_AGENT);
}
DI void wait_flags(const u32* p) {
  u32 spins = 0;
  while (!__all(__hip_atomic_load(p, __ATOMIC_RELAXED, __HIP_MEMORY_SCOPE_AGENT) != 0u)) {
    __builtin_amdgcn_s_sleep(1);
    if (++spins > (1u << 24)) break;
  }
  __builtin_amdgcn_fence(__ATOMIC_ACQUIRE, "wavefront");
}
DI u32 h_dword(float g0, float g1, float u0, float u1) {
  const u32 g = pack_bf(g0, g1), u = pack_bf(u0, u1);
  return pack_bf(bf_lo(g) * bf_lo(u), bf_hi(g) * bf_hi(u));
}
DI void poll_issue(u32& v, const u32* p) {
  asm volatile("global_load_dword %0, %1, off sc1" : "=v"(v) : "v"(p) : "memory");
}
DI void poll_check(u32& v, u32* p, u32 target) {
  asm volatile("" : "+v"(v));
  if (__builtin_amdgcn_readfirstlane(v) < target) wait_count(p, target);
}

DI void flags_check(u32& v, const u32* p) {
  asm volatile("" : "+v"(v));
  if (!__all(v != 0u)) wait_flags(p);
}

struct G2Dma {
  srd_t sw, sx;
  u32 wsoffA, wsoffB, xsoff, m0w, m0x, woff, soff, xoff;
};
DI void g2_piece(const G2Dma& d, int lane, int st, int i) {
  const u32 bo = g_slot(st);
  const int kb = st < 6 ? 6 * st : st < 12 ? 6 * (st - 6) + 1 : 6 * (st - 12) + 4;
  const int kb2 = kb + (st < 12 ? 2 : 1);
  const u32 wo = (st < 12 ? d.wsoffA : d.wsoffB) + (u32)kb * (32 * D_ * 2);
  if (i == 0) dma16(d.m0w + bo, d.woff, d.sw, wo);
  else if (i == 1) dma16(d.m0w + bo + 1024, d.woff, d.sw, wo + 128);
  else if (i == 2) { if (lane < 16) dma16(d.m0w + bo + 2048, d.soff, d.sw, wo + 256); }
  else if (i == 3) dma16_sc1(d.m0x + bo, d.xoff, d.sx, d.xsoff + kb * 1024);
  else dma16_sc1(d.m0x + bo + 1024, d.xoff, d.sx, d.xsoff + kb2 * 1024);
}

DI void gemm1_body(u32 sb, srd_t sw, srd_t sx, u32 wsoff, u32 xsoff, int w, int lane, int wm, int wn,
                   char* ws, u32 hoffA, u32* hsig, const G2Dma& g2, const u32* hflg, v4u (&hB)[4]) {
  STAMP(ws, 1, 0);
  FS_DECL
  FSTAMP(0);
  constexpr int NU = 8;
  const int g = lane >> 4, qq = (lane & 15) >> 2, p = lane & 3;
  const int rr = 4 * (g & 1) + qq;
  const int wl0 = rr * 128 + (2 * (rr >> 1)) * 16 + p * 8;
  const u32 woff = (u32)((lane >> 3) * (F_ * 2) + ((lane & 7) ^ (2 * (lane >> 4))) * 16);
  const u32 xoff = lane * 16;
  v8s wf[3], xf[2][4];
  const u32 m0wB = sb + (w & 3) * 2048 + (w >> 2) * 1024;
  const u32 m0xB = sb + 16384 + w * 4096;
  auto dmaB = [&](int st, int i) {
    const u32 bo = b_slot(st);
    if (i < 2) dma16(m0wB + bo + i * 8192, woff, sw, wsoff + (u32)st * (64 * F_ * 2) + 256 + i * (32 * F_ * 2));
    else dma16(m0xB + bo + (i - 2) * 1024, xoff, sx, xsoff + st * 2048 + ((i - 2) >> 1) * (18 * 1024) + ((i - 2) & 1) * 1024);
  };
  v4f accA[2][4][4];
  {
    u32 wp[4], xp;
#pragma unroll
    for (int t = 0; t < 4; ++t) wp[t] = opaque((sb + wl0 + (g >> 1) * 4096 + wn * 1024) ^ (t << 5));
    xp = opaque(sb + 16384 + wm * 4096 + lane * 16);
    const u32 m0w = sb + (w & 3) * 4096 + (w >> 2) * 2048;
    const u32 m0x = sb + 16384 + w * 2048;
    auto dma_piece = [&](int st, int i) {
      const u32 bo = (st & 3) * A_STAGE;
      if (i < 2) dma16(m0w + bo + i * 1024, woff, sw, wsoff + (u32)st * (32 * F_ * 2) + i * 128);
      else dma16(m0x + bo + (i - 2) * 1024, xoff, sx, xsoff + st * 1024 + (i - 2) * (18 * 1024));
    };
#pragma unroll
    for (int st = 0; st < 3; ++st)
#pragma unroll
      for (int i = 0; i < 4; ++i) dma_piece(st, i);
    wait_vm<8>();
    stage_barrier();
#pragma unroll
    for (int mb = 0; mb < 4; ++mb) xf[0][mb] = ld_frag(xp, mb * 1024);
    STAMP(ws, 1, 1);
    wf[0] = tr_frag(wp[0], 0, 8192);
    wf[1] = tr_frag(wp[0], 2048, 2048 + 8192);
    dma_piece(3, 0); dma_piece(3, 1);
    SCHED0();
    static_for<18 * NU>([&](auto UC) {
      constexpr int U = decltype(UC)::value, st = U / NU, u = U % NU;
      constexpr int T = u >> 1, gu = u & 1;
      constexpr bool trans = (u == NU - 2 && st + 1 < 18);
      constexpr bool rd = (u + 2 < NU) || (st + 1 < 18);
      constexpr int un = (u + 2) % NU;
      auto mm = [&](int mb) {
        if constexpr (st == 0) mfma16_first(accA[gu][T][mb], wf[U % 3], xf[st & 1][mb]);
        else mfma16(accA[gu][T][mb], wf[U % 3], xf[st & 1][mb]);
        SCHED0();
      };
      v4s lo, hi;
      mm(0);
      if constexpr (!trans && rd) { lo = tr_half(wp[un >> 1], (un & 1) * 2048); SCHED0(); }
      mm(1);
      if constexpr (trans) {
        if constexpr (st + 1 <= 15) wait_vm<8>(); else if constexpr (st + 1 == 16) wait_vm<4>(); else wait_vm<8>();
        stage_barrier();
        FSTAMP(1 + st);
        constexpr int dl = (((st + 1) & 3) == 0) ? -3 * A_STAGE : A_STAGE;
#pragma unroll
        for (int t = 0; t < 4; ++t) wp[t] = opaque(wp[t] + dl);
        xp = opaque(xp + dl);
        lo = tr_half(wp[un >> 1], (un & 1) * 2048);
      }
      if constexpr (rd) { hi = tr_half(wp[un >> 1], (un & 1) * 2048 + 8192); wf[(U + 2) % 3] = join(lo, hi); }
      SCHED0();
      mm(2);
      if constexpr (u >= NU - 2 && st + 1 < 18) {
        xf[(st + 1) & 1][2 * (u - 6)] = ld_frag(xp, (2 * (u - 6)) * 1024);
        xf[(st + 1) & 1][2 * (u - 6) + 1] = ld_frag(xp, (2 * (u - 6) + 1) * 1024);
      }
      SCHED0();
      mm(3);
      if constexpr (u >= NU - 2) { if constexpr (st + 4 < 18) dma_piece(st + 4, u - (NU - 2)); }
      else if constexpr (u < 2) { if constexpr (st + 3 < 18) dma_piece(st + 3, u + 2); }
      constexpr int qB = U - (15 * NU + 6);
      if constexpr (qB >= 0 && qB < 12) dmaB(qB / 6, qB % 6);
      SCHED0();
    });
  }
  FSTAMP(18);
  {
    v4f acc[2][2][4];
    v4u hA[8];
    u32 wp[2], xp, pollA = 0;
#pragma unroll
    for (int t = 0; t < 2; ++t) wp[t] = opaque((sb + b_slot(0) + wl0 + (g >> 1) * 2048) ^ ((2 * wn + t) << 5));
    xp = opaque(sb + b_slot(0) + 16384 + wm * 8192 + lane * 16);
    auto storeA = [&](int i) {
      __builtin_amdgcn_raw_buffer_store_b128(hA[i], g2.sx, lane * 16, hoffA + (i & 3) * (144 * 1024) + (i >> 2) * 1024, 16);
    };
    auto conv1 = [&](int cq) {
      const int gi = cq >> 2, pr = gi >> 2, mb = gi & 3, cT = 2 * pr + ((cq >> 1) & 1), ch = cq & 1;
      hA[gi][cq & 3] = h_dword(accA[0][cT][mb][2 * ch], accA[0][cT][mb][2 * ch + 1], accA[1][cT][mb][2 * ch], accA[1][cT][mb][2 * ch + 1]);
    };
    wait_vm<6>();
    stage_barrier();
#pragma unroll
    for (int mb = 0; mb < 4; ++mb) xf[0][mb] = ld_frag(xp, mb * 2048);
    wf[0] = tr_frag(wp[0], 0, 4096);
    wf[1] = tr_frag(wp[0], 1024, 1024 + 4096);
    dmaB(2, 0); dmaB(2, 1);
    mfma_drain();
    FSTAMP(19);
    SCHED0();
    static_for<9 * NU>([&](auto UC) {
      constexpr int U = decltype(UC)::value, st = U / NU, u = U % NU;
      constexpr int T = (u >> 1) & 1, gu = u & 1;
      constexpr bool trans = (u == NU - 2 && st + 1 < 9);
      constexpr bool rd = (u + 2 < NU) || (st + 1 < 9);
      constexpr int un = (u + 2) % NU;
      constexpr int wo = (un >> 2) * 8192 + (un & 1) * 1024;
      auto mm = [&](int mb) {
        if constexpr (st == 0 && u < 4) mfma16_first(acc[gu][T][mb], wf[U % 3], xf[u >> 2][mb]);
        else mfma16(acc[gu][T][mb], wf[U % 3], xf[u >> 2][mb]);
        SCHED0();
      };
      v4s lo, hi;
      mm(0);
      if constexpr (!trans && rd) { lo = tr_half(wp[(un >> 1) & 1], wo); }
      if constexpr (U < 16) conv1(2 * U);
      SCHED0();
      mm(1);
      if constexpr (trans) {
        wait_vm<gb_wait(st)>();
        stage_barrier();
        if constexpr (st == 4) h_signal(hsig, lane);
        if constexpr (st == 6) poll_issue(pollA, hflg);
        if constexpr (st == 7) { h_signal(hsig + 64 * 32, lane); flags_check(pollA, hflg); }
        FSTAMP(20 + st);
        constexpr int dl = b_slot(st + 1) - b_slot(st);
#pragma unroll
        for (int t = 0; t < 2; ++t) wp[t] = opaque(wp[t] + dl);
        xp = opaque(xp + dl);
        lo = tr_half(wp[(un >> 1) & 1], wo);
      }
      if constexpr (rd) { hi = tr_half(wp[(un >> 1) & 1], wo + 2 * 2048); wf[(U + 2) % 3] = join(lo, hi); }
      SCHED0();
      mm(2);
      if constexpr (u == 1 || u == 2) { xf[1][2 * (u - 1)] = ld_frag(xp, (2 * (u - 1)) * 2048 + 1024); xf[1][2 * (u - 1) + 1] = ld_frag(xp, (2 * (u - 1) + 1) * 2048 + 1024); }
      if constexpr (u >= NU - 2 && st + 1 < 9) { xf[0][2 * (u - 6)] = ld_frag(xp, (2 * (u - 6)) * 2048); xf[0][2 * (u - 6) + 1] = ld_frag(xp, (2 * (u - 6) + 1) * 2048); }
      if constexpr (U < 16) conv1(2 * U + 1);
      SCHED0();
      mm(3);
      if constexpr (u >= NU - 2) { if constexpr (st + 3 < 9) dmaB(st + 3, u - (NU - 2)); }
      else if constexpr (u < 4) { if constexpr (st + 2 < 9) dmaB(st + 2, u + 2); }
      if constexpr ((st == 1 || st == 2) && (u == 4 || u == 5)) storeA(2 * (st - 1) + u - 4);
      if constexpr (st >= 3 && st <= 6 && u == 4) storeA(st + 1);
      constexpr int qG = U - (7 * NU + 6);
      if constexpr (qG >= 0 && qG < 4) { g2_piece(g2, lane, (2 * qG) / 3, (2 * qG) % 3); g2_piece(g2, lane, (2 * qG + 1) / 3, (2 * qG + 1) % 3); }
      if constexpr (qG == 4) g2_piece(g2, lane, 2, 2);
      if constexpr (qG >= 5 && qG < 8) { g2_piece(g2, lane, qG - 5, 3); g2_piece(g2, lane, qG - 5, 4); }
      SCHED0();
    });
    mfma_drain();
    SCHED0();
    STAMP(ws, 1, 2);
    FSTAMP(28);
    FS_DUMP(ws, 29);
#pragma unroll
    for (int mb = 0; mb < 4; ++mb)
#pragma unroll
      for (int c = 0; c < 4; ++c)
        hB[mb][c] = h_dword(acc[0][c >> 1][mb][2 * (c & 1)], acc[0][c >> 1][mb][2 * (c & 1) + 1],
                            acc[1][c >> 1][mb][2 * (c & 1)], acc[1][c >> 1][mb][2 * (c & 1) + 1]);
  }
}

template <int NT, bool PRE>
DI void gemm2_body(char* smem, u32 sb, const G2Dma& g2, int w, int lane, int kg, int wm, int tbase, int tile, int ksl,
                   const u16* __restrict__ xin, u16* __restrict__ out, char* ws, u32* hsigB, const v4u (&hB)[4],
                   u32 hBoff) {
  constexpr int NU = NT;
  constexpr int NO = (2 * NT + 3) / 4;
  v4f acc[NT][4];
  STAMP(ws, 2, 0);
  FS_DECL
  FSTAMP(0);
  const int g = lane >> 4, qq = (lane & 15) >> 2, p = lane & 3;
  const int rr = 4 * (g & 1) + qq;
  const int wl0 = rr * 128 + (2 * (rr >> 1)) * 16 + p * 8;
  u32 wp[NT], xp;
#pragma unroll
  for (int t = 0; t < 4; ++t)
    wp[t] = opaque((sb + g_slot(0) + wl0 + ((g >> 1) + 4 * kg) * 2304 + (NT == 5 ? 1024 : 0)) ^ (t << 5));
  if constexpr (NT == 5) wp[4] = opaque(sb + g_slot(0) + 2048 + rr * 32 + p * 8 + ((g >> 1) + 4 * kg) * 2304);
  xp = opaque(sb + g_slot(0) + G_XOFF + wm * 8192 + kg * 1024 + lane * 16);
  auto rdW = [&](int u) -> v8s { return tr_frag(wp[u], 0, 2 * 2304); };
  const u32* hflg = (const u32*)(ws + WS_HCNT) + ((tile >> 2) * 4 + ksl) * 32 + (lane < 24 ? lane : 0);
  u32* ycnt = (u32*)(ws + WS_YCNT);
  v8s wf[3], xf[2][4];
  u32 pollB = 0, pollY = 0;
  const int j = 2 * wm + kg;
  const int tok0 = (tile >> 2) * 128 + j * 32 + (lane & 15) + 16 * (ksl & 1);
  const int dcol = (tile & 3) * 144 + tbase * 16 + (lane >> 4) * 4 + 16 * (ksl >> 1);
  v2u xv[NO]; u32 yv[NO][4]; v2f stt;
  constexpr int NL = 1 + 5 * NO;
  char* xw = smem + ((wm * 2 + kg) * 9 + tbase) * 2048 + lane * 16;
  if constexpr (!PRE) {
#pragma unroll
    for (int st = 0; st < 3; ++st)
#pragma unroll
      for (int i = 0; i < 3; ++i) g2_piece(g2, lane, st, i);
    wait_flags(hflg);
#pragma unroll
    for (int st = 0; st < 3; ++st) { g2_piece(g2, lane, st, 3); g2_piece(g2, lane, st, 4); }
  }
  wait_vm<4>();
  stage_barrier();
#pragma unroll
  for (int mb = 0; mb < 4; ++mb) xf[0][mb] = ld_frag(xp, mb * 2048);
  STAMP(ws, 2, 1);
  wf[0] = rdW(0);
  wf[1] = rdW(1);
  g2_piece(g2, lane, 3, 0); g2_piece(g2, lane, 3, 1);
  SCHED0();
  static_for<18 * NU>([&](auto UC) {
    constexpr int U = decltype(UC)::value, st = U / NU, u = U % NU;
    constexpr int SB = PRE ? 2 : 0;
    constexpr bool trans = (u == NU - 2 && st + 1 < 18);
    constexpr bool rd = (u + 2 < NU) || (st + 1 < 18);
    constexpr int un = (u + 2) % NU;
    auto mm = [&](int mb) {
      if constexpr (st == 0) mfma16_first(acc[u][mb], wf[U % 3], xf[st & 1][mb]);
      else mfma16(acc[u][mb], wf[U % 3], xf[st & 1][mb]);
      SCHED0();
    };
    v4s lo, hi;
    mm(0);
    if constexpr (!trans && rd) { lo = tr_half(wp[un], 0); SCHED0(); }
    mm(1);
    if constexpr (trans) {
      constexpr int NW = st == 0 ? 7 + SB : st == 1 ? 11 + 2 * SB : st == 2 ? 10 + SB : st == 3 ? 10 + SB
                         : st + 1 <= 15 ? 10 : st + 1 == 16 ? 5 : 0;
      wait_vm<NW>();
      stage_barrier();
      if constexpr (st == 0) poll_issue(pollB, hflg + 64 * 32);
      if constexpr (st == 2) flags_check(pollB, hflg + 64 * 32);
      if constexpr (PRE && st == 4) h_signal(hsigB, lane);
      if constexpr (st == 6) poll_issue(pollB, hflg + 128 * 32);
      if constexpr (st == 8) flags_check(pollB, hflg + 128 * 32);
      if constexpr (st == 12) poll_issue(pollY, ycnt);
      FSTAMP(1 + st);
      constexpr int dl = g_slot(st + 1) - g_slot(st);
#pragma unroll
      for (int t = 0; t < NT; ++t) wp[t] = opaque(wp[t] + dl);
      xp = opaque(xp + dl);
      lo = tr_half(wp[un], 0);
    }
    if constexpr (rd) { hi = tr_half(wp[un], 2 * 2304); wf[(U + 2) % 3] = join(lo, hi); }
    SCHED0();
    mm(2);
    if constexpr (u >= NU - 2 && st + 1 < 18) {
      xf[(st + 1) & 1][2 * (u - (NU - 2))] = ld_frag(xp, (2 * (u - (NU - 2))) * 2048);
      xf[(st + 1) & 1][2 * (u - (NU - 2)) + 1] = ld_frag(xp, (2 * (u - (NU - 2)) + 1) * 2048);
    }
    if constexpr (u == 0) { if constexpr (st + 3 < 18) g2_piece(g2, lane, st + 3, 2); }
    SCHED0();
    mm(3);
    if constexpr (u >= NU - 2) { if constexpr (st + 4 < 18) g2_piece(g2, lane, st + 4, u - (NU - 2)); }
    else if constexpr (u == 0) { if constexpr (st + 3 < 18) g2_piece(g2, lane, st + 3, 3); }
    else if constexpr (u == 1) { if constexpr (st + 3 < 18) g2_piece(g2, lane, st + 3, 4); }
    if constexpr (PRE && u == 1 && st < 2) {
#pragma unroll
      for (int mb = 2 * st; mb < 2 * st + 2; ++mb)
        __builtin_amdgcn_raw_buffer_store_b128(hB[mb], g2.sx, lane * 16, hBoff + mb * (144 * 1024), 16);
    }
    if constexpr (st == 17 && u >= 1) {
#pragma unroll
      for (int b2 = 0; b2 < 2; ++b2) *(v4f*)(xw + ((u - 1) * 2 + b2) * 1024) = kg ? acc[u - 1][b2] : acc[u - 1][2 + b2];
    }
    SCHED0();
  });
  STAMP(ws, 2, 2);
  FSTAMP(18);
  mfma_drain();
  SCHED0();
  FSTAMP(19);
#pragma unroll
  for (int b2 = 0; b2 < 2; ++b2) *(v4f*)(xw + ((NT - 1) * 2 + b2) * 1024) = kg ? acc[NT - 1][b2] : acc[NT - 1][2 + b2];
  stage_barrier();
  v4f r[2 * NT];
  {
    const char* rd = smem + ((wm * 2 + (kg ^ 1)) * 9 + tbase) * 2048 + lane * 16;
#pragma unroll
    for (int T = 0; T < NT; ++T)
#pragma unroll
      for (int b2 = 0; b2 < 2; ++b2) {
        const v4f o = *(const v4f*)(rd + (T * 2 + b2) * 1024);
        r[T * 2 + b2] = kg ? (o + acc[T][2 + b2]) : (acc[T][b2] + o);
      }
  }
  FSTAMP(20);
  const srd_t sp = make_srd(ws + WS_PART, 64 * 4 * 8 * 4 * 3072);
#pragma unroll
  for (int f = 0; f < 2 * NT; ++f)
    if ((f & 3) != ksl)
      __builtin_amdgcn_raw_buffer_store_b128(__builtin_bit_cast(v4u, r[f]), sp, lane * 16,
                                             (u32)((((tile * 4 + (f & 3)) * 8 + w) * 4 + ksl) * 3072 + (f >> 2) * 1024), 16);
  v4f own[NO];
#pragma unroll
  for (int i = 0; i < NO; ++i) {
    const v4f a0 = r[4 * i], a1 = r[4 * i + 1];
    const v4f a2 = (4 * i + 2 < 2 * NT) ? r[(4 * i + 2 < 2 * NT) ? 4 * i + 2 : 0] : a0;
    const v4f a3 = (4 * i + 3 < 2 * NT) ? r[(4 * i + 3 < 2 * NT) ? 4 * i + 3 : 0] : a0;
    own[i] = ksl == 0 ? a0 : ksl == 1 ? a1 : ksl == 2 ? a2 : a3;
  }
  FSTAMP(21);
  SCHED0();
  poll_check(pollY, ycnt, 576u);
  {
    const srd_t sy = make_srd(ws + WS_YT, S_ * D_ * 2);
    stt = ((const v2f*)(ws + WS_STATS))[tok0];
#pragma unroll
    for (int i = 0; i < NO; ++i) {
      const int dc = (4 * i + ksl < 2 * NT) ? dcol + 32 * i : dcol;
      xv[i] = *(const v2u*)(xin + (size_t)tok0 * D_ + dc);
#pragma unroll
      for (int r = 0; r < 4; ++r) yv[i][r] = __builtin_amdgcn_raw_buffer_load_b16(sy, (u32)(((dc + r) * S_ + tok0) * 2), 0, 16);
    }
  }
  SCHED0();
  wait_vm<NL>();
  FSTAMP(22);
  u32* flags = (u32*)(ws + WS_CNT);
  if (lane < 4 && lane != ksl)
    __hip_atomic_store(flags + ((tile * 4 + lane) * 8 + w) * 16 + ksl, 1u, __ATOMIC_RELAXED, __HIP_MEMORY_SCOPE_AGENT);
  STAMP(ws, 2, 3);
  float xa[NO][4];
#pragma unroll
  for (int i = 0; i < NO; ++i)
#pragma unroll
    for (int e = 0; e < 4; ++e) {
      const float xf_ = (e & 1) ? bf_hi(xv[i][e >> 1]) : bf_lo(xv[i][e >> 1]);
      const float xn = rbf(rbf(xf_ - stt[0]) * stt[1]);
      xa[i][e] = rbf(xf_ + rbf(xn * bf_lo(yv[i][e])));
    }
  {
    u32* fl = flags + ((tile * 4 + ksl) * 8 + w) * 16;
    u32 spins = 0;
    for (;;) {
      const u32 v = (lane < 4 && lane != ksl) ? __hip_atomic_load(fl + lane, __ATOMIC_RELAXED, __HIP_MEMORY_SCOPE_AGENT) : 1u;
      if (__all(v != 0u)) break;
      __builtin_amdgcn_s_sleep(1);
      if (++spins > (1u << 24)) break;
    }
  }
  __builtin_amdgcn_fence(__ATOMIC_ACQUIRE, "wavefront");
  STAMP(ws, 2, 4);
  FSTAMP(23);
  v4f pp[3][NO];
#pragma unroll
  for (int i3 = 0; i3 < 3; ++i3) {
    const int src = i3 + (i3 >= ksl ? 1 : 0);
    const u32 po = (u32)((((tile * 4 + ksl) * 8 + w) * 4 + src) * 3072);
#pragma unroll
    for (int i = 0; i < NO; ++i)
      pp[i3][i] = __builtin_bit_cast(v4f, __builtin_amdgcn_raw_buffer_load_b128(sp, lane * 16, po + i * 1024, 16));
  }
#pragma unroll
  for (int i = 0; i < NO; ++i) {
    const v4f s0 = ksl == 0 ? own[i] : pp[0][i];
    const v4f s1 = ksl == 1 ? own[i] : (ksl < 1 ? pp[0][i] : pp[1][i]);
    const v4f s2 = ksl == 2 ? own[i] : (ksl < 2 ? pp[1][i] : pp[2][i]);
    const v4f s3 = ksl == 3 ? own[i] : pp[2][i];
    const v4f B = (s0 + s1) + (s2 + s3);
    const float o0 = xa[i][0] + rbf(B[0]), o1 = xa[i][1] + rbf(B[1]);
    const float o2 = xa[i][2] + rbf(B[2]), o3 = xa[i][3] + rbf(B[3]);
    if (4 * i + ksl < 2 * NT)
      *(v2u*)(out + (size_t)tok0 * D_ + dcol + 32 * i) = v2u{pack_bf(o0, o1), pack_bf(o2, o3)};
  }
  STAMP(ws, 2, 5);
  FSTAMP(24);
  FS_DUMP2(ws, 25);
}

__global__ __launch_bounds__(512, 2) void fused_kernel(const u16* __restrict__ Wg, const u16* __restrict__ Wu,
                                                       const u16* __restrict__ Wd, const u16* __restrict__ ssmk,
                                                       const u16* __restrict__ ssmD, const u16* __restrict__ xin,
                                                       u16* __restrict__ out, char* __restrict__ ws) {
  extern __shared__ __attribute__((aligned(1024))) char smem[];
  const int bid = blockIdx.x;
  const int xcd = bid & 7, idx = bid >> 3;
  const int tid = threadIdx.x, lane = tid & 63;
  const int w = __builtin_amdgcn_readfirstlane(tid >> 6);
  const int ksl = xcd >> 1;
  const bool isconv = idx >= 24;
  const int tile2 = isconv ? (2 * (idx - 24) + (xcd & 1)) * 4 + 3 : (2 * (idx / 3) + (xcd & 1)) * 4 + idx % 3;
  const u32 sb = lds_addr(smem);
  G2Dma g2;
  g2.sw = make_srd(Wd, D_ * F_ * 2);
  g2.sx = make_srd(ws + WS_HT, S_ * F_ * 2);
  g2.wsoffA = (u32)((ksl * 1152 + 8 * (w & 3) + (w >> 2) * 64) * (D_ * 2) + (tile2 & 3) * 288);
  g2.wsoffB = (u32)((ksl * 1152 + 8 * w) * (D_ * 2) + (tile2 & 3) * 288);
  g2.xsoff = (u32)((((tile2 >> 2) * 8 + w) * 144 + ksl * 36) * 1024);
  g2.m0w = sb + w * 2304;
  g2.m0x = sb + G_XOFF + w * 2048;
  g2.woff = (u32)((lane >> 3) * (D_ * 2) + ((lane & 7) ^ (2 * (lane >> 4))) * 16);
  g2.soff = (u32)(((lane >> 1) & 7) * (D_ * 2) + (lane & 1) * 16);
  g2.xoff = lane * 16;
  const int kg2 = w >> 2, wm2 = (w >> 1) & 1, wn2 = (w & 1) ^ kg2;
  v4u hB[4];
  if (isconv) {
    const int cb = xcd * 8 + (idx - 24);
    STAMP(ws, 3, 0);
#pragma unroll 1
    for (int c = w; c < 9 && w < 7; c += 7) {
      conv_channel(smem + w * C_WAVE, cb * 9 + c, lane, ssmk, ssmD, ws);
      STAMP(ws, 3, 1 + c / 7);
    }
    stage_barrier();
#pragma unroll
    for (int i = 0; i < 4; ++i) hB[i] = v4u{0u, 0u, 0u, 0u};
    if (wn2 == 0) gemm2_body<5, false>(smem, sb, g2, w, lane, kg2, wm2, 4, tile2, ksl, xin, out, ws, nullptr, hB, 0u);
    else          gemm2_body<4, false>(smem, sb, g2, w, lane, kg2, wm2, 0, tile2, ksl, xin, out, ws, nullptr, hB, 0u);
  } else {
    const int tile_n = xcd * 3 + idx % 3, tile_m = idx / 3;
    const int wm = w & 3, wn = w >> 2;
    const srd_t sw = make_srd((w >> 2) ? Wu : Wg, D_ * F_ * 2);
    const srd_t sx = make_srd(ws + WS_XNT, S_ * D_ * 2);
    const u32 wsoff = (u32)((8 * (w & 3)) * (F_ * 2) + tile_n * 384);
    const u32 xsoff = (u32)((tile_m * 16 + 2 * w) * (18 * 1024));
    u32* hsigA = (u32*)(ws + WS_HCNT) + ((2 * tile_m + (wm >> 1)) * 4 + ksl) * 32 + (tile_n % 6) * 4 + (wm & 1) * 2 + wn;
    const u32* hflg = (const u32*)(ws + WS_HCNT) + ((tile2 >> 2) * 4 + ksl) * 32 + (lane < 24 ? lane : 0);
    const u32 hoff = (u32)(((tile_m * 16 + wm * 4) * 144 + tile_n * 6) * 1024);
    gemm1_body(sb, sw, sx, wsoff, xsoff, w, lane, wm, wn, ws, hoff + 2 * wn * 1024, hsigA, g2, hflg, hB);
    const u32 hBoff = hoff + (4 + wn) * 1024;
    if (wn2 == 0) gemm2_body<5, true>(smem, sb, g2, w, lane, kg2, wm2, 4, tile2, ksl, xin, out, ws, hsigA + 128 * 32, hB, hBoff);
    else          gemm2_body<4, true>(smem, sb, g2, w, lane, kg2, wm2, 0, tile2, ksl, xin, out, ws, hsigA + 128 * 32, hB, hBoff);
  }
}

extern "C" void kernel_launch(void* const* d_in, const int* in_sizes, int n_in, void* d_out, int out_size,
                              void* d_ws, size_t ws_size, hipStream_t stream) {
  const u16* q = (const u16*)d_in[0];
  const u16* k = (const u16*)d_in[1];
  const u16* v = (const u16*)d_in[2];
  const u16* shk = (const u16*)d_in[3];
  const u16* ssmk = (const u16*)d_in[4];
  const u16* ssmD = (const u16*)d_in[5];
  const u16* Wg = (const u16*)d_in[7];
  const u16* Wu = (const u16*)d_in[8];
  const u16* Wd = (const u16*)d_in[9];
  char* ws = (char*)d_ws;
  (void)hipFuncSetAttribute((const void*)fused_kernel, hipFuncAttributeMaxDynamicSharedMemorySize, H_LDS);
  pre_kernel<<<256 + 288, 256, 0, stream>>>(q, k, v, shk, ws);
  fused_kernel<<<256, 512, H_LDS, stream>>>(Wg, Wu, Wd, ssmk, ssmD, q, (u16*)d_out, ws);
}
```

```cpp
#include <hip/hip_runtime.h>
#include <hip/hip_bf16.h>
#include <stdint.h>
#include <utility>

typedef unsigned short u16;
typedef unsigned int u32;
typedef short v4s __attribute__((ext_vector_type(4)));
typedef short v8s __attribute__((ext_vector_type(8)));
typedef float v4f __attribute__((ext_vector_type(4)));
typedef float v2f __attribute__((ext_vector_type(2)));
typedef u32 v4u __attribute__((ext_vector_type(4)));
typedef u32 v2u __attribute__((ext_vector_type(2)));
typedef __bf16 v2bf __attribute__((ext_vector_type(2)));
typedef __attribute__((address_space(3))) v4s* lds_v4s_ptr;

constexpr int S_ = 2048, D_ = 576, F_ = 4608;

#define DI __device__ __forceinline__

template <int... I, class Fn>
DI void static_for_impl(std::integer_sequence<int, I...>, Fn&& f) { (f(std::integral_constant<int, I>{}), ...); }
template <int N, class Fn>
DI void static_for(Fn&& f) { static_for_impl(std::make_integer_sequence<int, N>{}, f); }

DI float bf_lo(u32 w) { return __uint_as_float(w << 16); }
DI float bf_hi(u32 w) { return __uint_as_float(w & 0xffff0000u); }
DI float rbf(float f) { return (float)(__bf16)f; }
DI u32 pack_bf(float lo, float hi) {
  v2f t = {lo, hi};
  v2bf b = __builtin_convertvector(t, v2bf);
  return __builtin_bit_cast(u32, b);
}

template <int CTRL> DI float dpp_f(float v) {
  return __builtin_bit_cast(float, __builtin_amdgcn_update_dpp(0, __builtin_bit_cast(int, v), CTRL, 0xf, 0xf, false));
}
DI float sum32(float v) {
  v += dpp_f<0x128>(v);
  v += dpp_f<0x124>(v);
  v += dpp_f<0x122>(v);
  v += dpp_f<0x121>(v);
  return v + __shfl_xor(v, 16);
}
#define STAMP(ws, kid, idx)
#define FS_DECL
#define FSTAMP(i)
#define FS_DUMP(ws, n)
#define FS_DUMP2(ws, n)
constexpr size_t WS_XNT = 0;
constexpr size_t WS_STATS = WS_XNT + (size_t)S_ * D_ * 2;
constexpr size_t WS_KVT = WS_STATS + (size_t)S_ * 8;
constexpr size_t WS_YT = WS_KVT + (size_t)S_ * D_ * 2;
constexpr size_t WS_HT = WS_YT + (size_t)S_ * D_ * 2;
constexpr size_t WS_PART = WS_HT + (size_t)S_ * F_ * 2;
constexpr size_t WS_CNT = WS_PART + (size_t)64 * 4 * 8 * 4 * 3072;
constexpr size_t WS_HCNT = WS_CNT + 2048 * 64;
constexpr size_t WS_YCNT = WS_HCNT + 3 * 64 * 128;

__global__ __launch_bounds__(256) void pre_kernel(const u16* __restrict__ x, const u16* __restrict__ kin,
                                                  const u16* __restrict__ vin, const u16* __restrict__ shk,
                                                  char* __restrict__ ws) {
  const int lane = threadIdx.x & 63, w = threadIdx.x >> 6;
  STAMP(ws, 0, 0);
  if (blockIdx.x < 8) *(v4u*)(ws + WS_CNT + (size_t)(blockIdx.x * 256 + threadIdx.x) * 64) = v4u{0u, 0u, 0u, 0u};
  else if (blockIdx.x < 15) *(v4u*)(ws + WS_HCNT + (size_t)((blockIdx.x - 8) * 256 + threadIdx.x) * 16) = v4u{0u, 0u, 0u, 0u};
  if (blockIdx.x < 256) {
    const int c = lane & 31;
    const int row = blockIdx.x * 8 + w * 2 + (lane >> 5);
    const v4u* src = (const v4u*)(x + (size_t)row * D_);
    v4u d[3];
    d[0] = src[c]; d[1] = src[c + 32]; d[2] = src[64 + (c & 7)];
    const float lastw = (c < 8) ? 1.f : 0.f;
    float f[3][8];
#pragma unroll
    for (int i = 0; i < 3; ++i)
#pragma unroll
      for (int j = 0; j < 4; ++j) { f[i][2 * j] = bf_lo(d[i][j]); f[i][2 * j + 1] = bf_hi(d[i][j]); }
    float s0 = 0.f, s1 = 0.f, s2 = 0.f;
#pragma unroll
    for (int j = 0; j < 8; ++j) { s0 += f[0][j]; s1 += f[1][j]; s2 += f[2][j]; }
    const float mean = sum32(s0 + s1 + s2 * lastw) * (1.0f / D_);
    float q0 = 0.f, q1 = 0.f, q2 = 0.f;
#pragma unroll
    for (int j = 0; j < 8; ++j) {
      const float a = f[0][j] - mean, b = f[1][j] - mean, e = f[2][j] - mean;
      q0 += a * a; q1 += b * b; q2 += e * e;
    }
    const float var = sum32(q0 + q1 + q2 * lastw) * (1.0f / D_);
    const float mb = rbf(mean);
    const float sd = rbf(sqrtf(rbf(rbf(var) + 1e-5f)));
    const float rinv = 1.0f / sd;
    if (c == 0) ((v2f*)(ws + WS_STATS))[row] = v2f{mb, rinv};
    char* xnt = ws + WS_XNT;
    const int tb = row >> 4, m = row & 15;
    const int cc = c & 3, T = cc >> 1, q0i = 2 * (cc & 1);
#pragma unroll
    for (int i = 0; i < 3; ++i) {
      u32 o[4];
#pragma unroll
      for (int j = 0; j < 4; ++j) {
        const u32 dd = pack_bf(f[i][2 * j] - mb, f[i][2 * j + 1] - mb);
        o[j] = pack_bf(bf_lo(dd) * rinv, bf_hi(dd) * rinv);
      }
      const int kb = 8 * i + (c >> 2);
      char* base = xnt + ((size_t)(tb * 18 + kb) * 64 + (q0i * 16 + m)) * 16 + 8 * T;
      if (i < 2 || c < 8) {
        *(v2u*)(base) = v2u{o[0], o[1]};
        *(v2u*)(base + 256) = v2u{o[2], o[3]};
      }
    }
  } else {
    const int b = blockIdx.x - 256;
    const int tt = b / 9, ct = b % 9;
    const int tg = lane >> 5, cx = lane & 31;
    const int s0 = tt * 64 + w * 16 + tg * 8, d0 = ct * 64 + cx * 2;
    u32 kk[11], vv[8];
#pragma unroll
    for (int i = 0; i < 11; ++i) {
      const int s = s0 - 3 + i;
      const u32 t = *(const u32*)(kin + (size_t)(s < 0 ? 0 : s) * D_ + d0);
      kk[i] = (i < 3 && s < 0) ? 0u : t;
    }
#pragma unroll
    for (int i = 0; i < 8; ++i) vv[i] = *(const u32*)(vin + (size_t)(s0 + i) * D_ + d0);
    const v4u w0 = *(const v4u*)(shk + (size_t)d0 * 4);
    u16* kvt = (u16*)(ws + WS_KVT);
#pragma unroll
    for (int ch = 0; ch < 2; ++ch) {
      const u32 wa = w0[2 * ch], wb = w0[2 * ch + 1];
      const float t0 = bf_lo(wa), t1 = bf_hi(wa), t2 = bf_lo(wb), t3 = bf_hi(wb);
      float kf[11];
#pragma unroll
      for (int i = 0; i < 11; ++i) kf[i] = ch ? bf_hi(kk[i]) : bf_lo(kk[i]);
      float r[8];
#pragma unroll
      for (int i = 0; i < 8; i += 2) {
        u32 a = pack_bf(kf[i] * t3, kf[i + 1] * t3);
        u32 p = pack_bf(kf[i + 1] * t2, kf[i + 2] * t2);
        a = pack_bf(bf_lo(a) + bf_lo(p), bf_hi(a) + bf_hi(p));
        p = pack_bf(kf[i + 2] * t1, kf[i + 3] * t1);
        a = pack_bf(bf_lo(a) + bf_lo(p), bf_hi(a) + bf_hi(p));
        p = pack_bf(kf[i + 3] * t0, kf[i + 4] * t0);
        a = pack_bf(bf_lo(a) + bf_lo(p), bf_hi(a) + bf_hi(p));
        r[i] = bf_lo(a) * (ch ? bf_hi(vv[i]) : bf_lo(vv[i]));
        r[i + 1] = bf_hi(a) * (ch ? bf_hi(vv[i + 1]) : bf_lo(vv[i + 1]));
      }
      v4u o = {pack_bf(r[7], r[6]), pack_bf(r[5], r[4]), pack_bf(r[3], r[2]), pack_bf(r[1], r[0])};
      *(v4u*)(kvt + (size_t)(d0 + ch) * S_ + s0) = o;
    }
  }
  STAMP(ws, 0, 1);
}

constexpr int G_STAGE = 34816, G_NBUF = 4, G_LDS = G_STAGE * G_NBUF;
constexpr int G_XOFF = 18432;

typedef __attribute__((address_space(3))) v8s* lds_v8s_ptr;
typedef __attribute__((address_space(3))) char* lds_ptr;
DI v8s tr_frag(u32 p, int off_lo, int off_hi) {
  v4s a = __builtin_amdgcn_ds_read_tr16_b64_v4i16((lds_v4s_ptr)((lds_ptr)p + off_lo));
  v4s b = __builtin_amdgcn_ds_read_tr16_b64_v4i16((lds_v4s_ptr)((lds_ptr)p + off_hi));
  return __builtin_shufflevector(a, b, 0, 1, 2, 3, 4, 5, 6, 7);
}
DI v4s tr_half(u32 p, int off) { return __builtin_amdgcn_ds_read_tr16_b64_v4i16((lds_v4s_ptr)((lds_ptr)p + off)); }
DI v8s join(const v4s& a, const v4s& b) { return __builtin_shufflevector(a, b, 0, 1, 2, 3, 4, 5, 6, 7); }
DI v8s ld_frag(u32 p, int off) { return *(lds_v8s_ptr)((lds_ptr)p + off); }
DI u32 opaque(u32 v) { asm volatile("" : "+v"(v)); return v; }
DI u32 lds_addr(const void* p) { return (u32)(size_t)(lds_ptr)p; }
typedef __amdgpu_buffer_rsrc_t srd_t;
DI srd_t make_srd(const void* p, u32 bytes) {
  return __builtin_amdgcn_make_buffer_rsrc((void*)p, 0, bytes, 0x00020000);
}
DI void dma16(u32 m0v, u32 voff, srd_t srd, u32 soff) {
  asm volatile("s_mov_b32 m0, %0\n\ts_nop 0\n\tbuffer_load_dwordx4 %1, %2, %3 offen lds"
               :: "s"(m0v), "v"(voff), "s"(srd), "s"(soff) : "memory");
}
DI void dma16_sc1(u32 m0v, u32 voff, srd_t srd, u32 soff) {
  asm volatile("s_mov_b32 m0, %0\n\ts_nop 0\n\tbuffer_load_dwordx4 %1, %2, %3 offen sc1 lds"
               :: "s"(m0v), "v"(voff), "s"(srd), "s"(soff) : "memory");
}
DI void wait_count(u32* p, u32 target) {
  u32 spins = 0;
  while (__hip_atomic_load(p, __ATOMIC_RELAXED, __HIP_MEMORY_SCOPE_AGENT) < target) {
    __builtin_amdgcn_s_sleep(1);
    if (++spins > (1u << 24)) break;
  }
  __builtin_amdgcn_fence(__ATOMIC_ACQUIRE, "wavefront");
}
#define SCHED0() __builtin_amdgcn_sched_barrier(0)
DI void mfma16(v4f& c, const v8s& a, const v8s& b) {
  asm volatile("v_mfma_f32_16x16x32_bf16 %0, %1, %2, %0" : "+v"(c) : "v"(a), "v"(b));
}
DI void mfma16_first(v4f& c, const v8s& a, const v8s& b) {
  asm volatile("v_mfma_f32_16x16x32_bf16 %0, %1, %2, 0" : "=&v"(c) : "v"(a), "v"(b));
}
DI void mfma_drain() { asm volatile("s_nop 15\n\ts_nop 15" ::: "memory"); }
template <int N> DI void wait_vm() { asm volatile("s_waitcnt vmcnt(%0)" :: "n"(N) : "memory"); }
DI void stage_barrier() { asm volatile("s_waitcnt lgkmcnt(0)\n\ts_barrier" ::: "memory"); }

constexpr int C_KBYTES = 2080 * 8, C_XBYTES = 8 * 768, C_WAVE = C_KBYTES + C_XBYTES;

DI void conv_channel(char* kl, int ch, int lane, const u16* __restrict__ ssmk, const u16* __restrict__ ssmD,
                     char* __restrict__ ws) {
  char* xl = kl + C_KBYTES;
  const srd_t sk = make_srd(ssmk + (size_t)ch * S_, S_ * 2);
  const v4u* xs = (const v4u*)(ws + WS_KVT + (size_t)ch * S_ * 2);
  v4u xd[4];
#pragma unroll
  for (int it = 0; it < 4; ++it) xd[it] = xs[it * 64 + lane];
  u32 d0[17], d1[17], d2[17];
#pragma unroll
  for (int it = 0; it < 17; ++it) {
    const int dw = it * 64 + lane - 16;
    if (it == 0 || it == 16) {
      d0[it] = __builtin_amdgcn_raw_buffer_load_b32(sk, (u32)(dw * 4), 0, 0);
      d1[it] = __builtin_amdgcn_raw_buffer_load_b32(sk, (u32)(dw * 4 + 4), 0, 0);
      d2[it] = __builtin_amdgcn_raw_buffer_load_b32(sk, (u32)(dw * 4 + 8), 0, 0);
    } else {
      typedef u32 v3u __attribute__((ext_vector_type(3)));
      const v3u t = __builtin_amdgcn_raw_buffer_load_b96(sk, (u32)(dw * 4), 0, 0);
      d0[it] = t[0]; d1[it] = t[1]; d2[it] = t[2];
    }
  }
#pragma unroll
  for (int it = 0; it < 2; ++it) {
    const int z = it * 64 + lane;
    *(v4u*)(xl + (z >> 4) * 768 + (z & 15) * 16) = v4u{0u, 0u, 0u, 0u};
  }
#pragma unroll
  for (int it = 0; it < 4; ++it) {
    const int cidx = it * 64 + lane;
    *(v4u*)(xl + (cidx & 7) * 768 + ((cidx >> 3) + 16) * 16) = xd[it];
  }
#pragma unroll
  for (int it = 0; it < 17; ++it) {
    const int L = it * 64 + lane;
    v4u o = {d0[it], d1[it], __builtin_amdgcn_alignbit(d1[it], d0[it], 16), __builtin_amdgcn_alignbit(d2[it], d1[it], 16)};
    if (it < 16 || lane < 16) *(v4u*)(kl + L * 16) = o;
  }
  __builtin_amdgcn_s_waitcnt(0xC07F);
  __builtin_amdgcn_wave_barrier();

  const int i16 = lane & 15, q = lane >> 4;
  const u32 ap = opaque(lds_addr(kl) + (i16 - 8 * q - 7 + 32) * 8);
  const u32 bp = opaque(lds_addr(xl) + q * 768 + i16 * 16);
  v4f acc[4][2];
  v8s af[3][4], bfr[2][4];
  auto rdA = [&](int c, int j) -> v8s {
    const int delta = 64 * c + 16 * j;
    v4s lo = *(volatile __attribute__((address_space(3))) v4s*)((lds_ptr)ap + 8 * delta);
    v4s hi = *(volatile __attribute__((address_space(3))) v4s*)((lds_ptr)ap + 8 * delta + 32);
    return __builtin_shufflevector(lo, hi, 0, 1, 2, 3, 4, 5, 6, 7);
  };
  auto rdB = [&](int c, int n, int kb) -> v8s { return ld_frag(bp, 4 * kb * 768 + 16 * (16 * n - c + 16)); };
#pragma unroll
  for (int j = 0; j < 4; ++j) af[0][j] = rdA(0, j);
#pragma unroll
  for (int j = 0; j < 4; ++j) bfr[0][j] = rdB(0, j >> 1, j & 1);
  SCHED0();
  static_for<32>([&](auto CC) {
    constexpr int c = decltype(CC)::value;
    if constexpr (c + 1 < 32) {
#pragma unroll
      for (int j = 0; j < 4; ++j) af[(c + 1) % 3][j] = rdA(c + 1, j);
#pragma unroll
      for (int j = 0; j < 4; ++j)
        if (j >= 2 || c + 1 < 16) bfr[(c + 1) & 1][j] = rdB(c + 1, j >> 1, j & 1);
    }
    SCHED0();
#pragma unroll
    for (int n = 1; n >= 0; --n) {
      if (n == 0 && c >= 16) continue;
#pragma unroll
      for (int kb = 0; kb < 2; ++kb) {
#pragma unroll
        for (int ib = 0; ib < 4; ++ib) {
          const int j = ib - 2 * kb;
          if (c == 0 && j < 0) continue;
          const v8s& A = (j >= 0) ? af[c % 3][j >= 0 ? j : 0] : af[(c + 2) % 3][j < 0 ? j + 4 : 0];
          if (c == 0 && kb == 0) mfma16_first(acc[ib][n], A, bfr[c & 1][2 * n + kb]);
          else mfma16(acc[ib][n], A, bfr[c & 1][2 * n + kb]);
        }
      }
    }
    SCHED0();
  });
  mfma_drain();
  SCHED0();
  const float Dd = bf_lo((u32)ssmD[ch]);
  const srd_t sy = make_srd(ws + WS_YT + (size_t)ch * S_ * 2, S_ * 2);
#pragma unroll
  for (int n = 0; n < 2; ++n)
#pragma unroll
    for (int ib = 0; ib < 4; ++ib) {
      const int beta = 16 * n + i16;
      const v2u kvp = *(const v2u*)(xl + (2 * ib + (q >> 1)) * 768 + (beta + 16) * 16 + 8 * (1 - (q & 1)));
      const float k3 = bf_lo(kvp[0]), k2 = bf_hi(kvp[0]), k1 = bf_lo(kvp[1]), k0 = bf_hi(kvp[1]);
      const float y0 = rbf(acc[ib][n][0]) + rbf(k0 * Dd);
      const float y1 = rbf(acc[ib][n][1]) + rbf(k1 * Dd);
      const float y2 = rbf(acc[ib][n][2]) + rbf(k2 * Dd);
      const float y3 = rbf(acc[ib][n][3]) + rbf(k3 * Dd);
      __builtin_amdgcn_raw_buffer_store_b64(v2u{pack_bf(y0, y1), pack_bf(y2, y3)}, sy, (u32)((64 * beta + 16 * ib + 4 * q) * 2), 0, 16);
    }
  asm volatile("s_waitcnt vmcnt(0) lgkmcnt(0)" ::: "memory");
  if (lane == 0) __hip_atomic_fetch_add((u32*)(ws + WS_YCNT), 1u, __ATOMIC_RELAXED, __HIP_MEMORY_SCOPE_AGENT);
  __builtin_amdgcn_wave_barrier();
}

constexpr int H_LDS = 163840;
constexpr int A_STAGE = 32768;
constexpr int b_slot(int s) { return (s % 3) == 0 ? 65536 : (s % 3) == 1 ? 114688 : 0; }
constexpr int g_slot(int s) { return (s & 3) == 0 ? 49152 : (s & 3) == 1 ? 83968 : (s & 3) == 2 ? 118784 : 0; }
constexpr int gb_store(int st) { return st == 1 ? 2 : st == 2 ? 2 : (st >= 3 && st <= 6) ? 1 : 0; }
constexpr int gb_extra(int st) { return (st == 4 || st == 6) ? 1 : 0; }
constexpr int gb_wait(int st) {
  return st >= 7 ? 0 : (st > 0 ? gb_store(st - 1) + gb_extra(st - 1) : 0) + 6 + gb_store(st);
}

DI void h_signal(u32* p, int lane) {
  if (lane == 0) __hip_atomic_store(p, 1u, __ATOMIC_RELAXED, __HIP_MEMORY_SCOPE_AGENT);
}
DI void wait_flags(const u32* p) {
  u32 spins = 0;
  while (!__all(__hip_atomic_load(p, __ATOMIC_RELAXED, __HIP_MEMORY_SCOPE_AGENT) != 0u)) {
    __builtin_amdgcn_s_sleep(1);
    if (++spins > (1u << 24)) break;
  }
  __builtin_amdgcn_fence(__ATOMIC_ACQUIRE, "wavefront");
}
DI u32 h_dword(float g0, float g1, float u0, float u1) {
  const u32 g = pack_bf(g0, g1), u = pack_bf(u0, u1);
  return pack_bf(bf_lo(g) * bf_lo(u), bf_hi(g) * bf_hi(u));
}
DI void poll_issue(u32& v, const u32* p) {
  asm volatile("global_load_dword %0, %1, off sc1" : "=v"(v) : "v"(p) : "memory");
}
DI void poll_check(u32& v, u32* p, u32 target) {
  asm volatile("" : "+v"(v));
  if (__builtin_amdgcn_readfirstlane(v) < target) wait_count(p, target);
}

DI void flags_check(u32& v, const u32* p) {
  asm volatile("" : "+v"(v));
  if (!__all(v != 0u)) wait_flags(p);
}

struct G2Dma {
  srd_t sw, sx;
  u32 wsoffA, wsoffB, xsoff, m0w, m0x, woff, soff, xoff;
};
DI void g2_piece(const G2Dma& d, int lane, int st, int i) {
  const u32 bo = g_slot(st);
  const int kb = st < 6 ? 6 * st : st < 12 ? 6 * (st - 6) + 1 : 6 * (st - 12) + 4;
  const int kb2 = kb + (st < 12 ? 2 : 1);
  const u32 wo = (st < 12 ? d.wsoffA : d.wsoffB) + (u32)kb * (32 * D_ * 2);
  if (i == 0) dma16(d.m0w + bo, d.woff, d.sw, wo);
  else if (i == 1) dma16(d.m0w + bo + 1024, d.woff, d.sw, wo + 128);
  else if (i == 2) { if (lane < 16) dma16(d.m0w + bo + 2048, d.soff, d.sw, wo + 256); }
  else if (i == 3) dma16_sc1(d.m0x + bo, d.xoff, d.sx, d.xsoff + kb * 1024);
  else dma16_sc1(d.m0x + bo + 1024, d.xoff, d.sx, d.xsoff + kb2 * 1024);
}

DI void gemm1_body(u32 sb, srd_t sw, srd_t sx, u32 wsoff, u32 xsoff, int w, int lane, int wm, int wn,
                   char* ws, u32 hoffA, u32* hsig, const G2Dma& g2, const u32* hflg, v4u (&hB)[4]) {
  STAMP(ws, 1, 0);
  FS_DECL
  FSTAMP(0);
  constexpr int NU = 8;
  const int g = lane >> 4, qq = (lane & 15) >> 2, p = lane & 3;
  const int rr = 4 * (g & 1) + qq;
  const int wl0 = rr * 128 + (2 * (rr >> 1)) * 16 + p * 8;
  const u32 woff = (u32)((lane >> 3) * (F_ * 2) + ((lane & 7) ^ (2 * (lane >> 4))) * 16);
  const u32 xoff = lane * 16;
  v8s wf[3], xf[2][4];
  const u32 m0wB = sb + (w & 3) * 2048 + (w >> 2) * 1024;
  const u32 m0xB = sb + 16384 + w * 4096;
  auto dmaB = [&](int st, int i) {
    const u32 bo = b_slot(st);
    if (i < 2) dma16(m0wB + bo + i * 8192, woff, sw, wsoff + (u32)st * (64 * F_ * 2) + 256 + i * (32 * F_ * 2));
    else dma16(m0xB + bo + (i - 2) * 1024, xoff, sx, xsoff + st * 2048 + ((i - 2) >> 1) * (18 * 1024) + ((i - 2) & 1) * 1024);
  };
  v4f accA[2][4][4];
  {
    u32 wp[4], xp;
#pragma unroll
    for (int t = 0; t < 4; ++t) wp[t] = opaque((sb + wl0 + (g >> 1) * 4096 + wn * 1024) ^ (t << 5));
    xp = opaque(sb + 16384 + wm * 4096 + lane * 16);
    const u32 m0w = sb + (w & 3) * 4096 + (w >> 2) * 2048;
    const u32 m0x = sb + 16384 + w * 2048;
    auto dma_piece = [&](int st, int i) {
      const u32 bo = (st & 3) * A_STAGE;
      if (i < 2) dma16(m0w + bo + i * 1024, woff, sw, wsoff + (u32)st * (32 * F_ * 2) + i * 128);
      else dma16(m0x + bo + (i - 2) * 1024, xoff, sx, xsoff + st * 1024 + (i - 2) * (18 * 1024));
    };
#pragma unroll
    for (int st = 0; st < 3; ++st)
#pragma unroll
      for (int i = 0; i < 4; ++i) dma_piece(st, i);
    wait_vm<8>();
    stage_barrier();
#pragma unroll
    for (int mb = 0; mb < 4; ++mb) xf[0][mb] = ld_frag(xp, mb * 1024);
    STAMP(ws, 1, 1);
    wf[0] = tr_frag(wp[0], 0, 8192);
    wf[1] = tr_frag(wp[0], 2048, 2048 + 8192);
    dma_piece(3, 0); dma_piece(3, 1);
    SCHED0();
    static_for<18 * NU>([&](auto UC) {
      constexpr int U = decltype(UC)::value, st = U / NU, u = U % NU;
      constexpr int T = u >> 1, gu = u & 1;
      constexpr bool trans = (u == NU - 2 && st + 1 < 18);
      constexpr bool rd = (u + 2 < NU) || (st + 1 < 18);
      constexpr int un = (u + 2) % NU;
      auto mm = [&](int mb) {
        if constexpr (st == 0) mfma16_first(accA[gu][T][mb], wf[U % 3], xf[st & 1][mb]);
        else mfma16(accA[gu][T][mb], wf[U % 3], xf[st & 1][mb]);
        SCHED0();
      };
      v4s lo, hi;
      mm(0);
      if constexpr (!trans && rd) { lo = tr_half(wp[un >> 1], (un & 1) * 2048); SCHED0(); }
      mm(1);
      if constexpr (trans) {
        if constexpr (st + 1 <= 15) wait_vm<8>(); else if constexpr (st + 1 == 16) wait_vm<4>(); else wait_vm<8>();
        stage_barrier();
        FSTAMP(1 + st);
        constexpr int dl = (((st + 1) & 3) == 0) ? -3 * A_STAGE : A_STAGE;
#pragma unroll
        for (int t = 0; t < 4; ++t) wp[t] = opaque(wp[t] + dl);
        xp = opaque(xp + dl);
        lo = tr_half(wp[un >> 1], (un & 1) * 2048);
      }
      if constexpr (rd) { hi = tr_half(wp[un >> 1], (un & 1) * 2048 + 8192); wf[(U + 2) % 3] = join(lo, hi); }
      SCHED0();
      mm(2);
      if constexpr (u >= NU - 2 && st + 1 < 18) {
        xf[(st + 1) & 1][2 * (u - 6)] = ld_frag(xp, (2 * (u - 6)) * 1024);
        xf[(st + 1) & 1][2 * (u - 6) + 1] = ld_frag(xp, (2 * (u - 6) + 1) * 1024);
      }
      SCHED0();
      mm(3);
      if constexpr (u >= NU - 2) { if constexpr (st + 4 < 18) dma_piece(st + 4, u - (NU - 2)); }
      else if constexpr (u < 2) { if constexpr (st + 3 < 18) dma_piece(st + 3, u + 2); }
      constexpr int qB = U - (15 * NU + 6);
      if constexpr (qB >= 0 && qB < 12) dmaB(qB / 6, qB % 6);
      SCHED0();
    });
  }
  FSTAMP(18);
  {
    v4f acc[2][2][4];
    v4u hA[8];
    u32 wp[2], xp, pollA = 0;
#pragma unroll
    for (int t = 0; t < 2; ++t) wp[t] = opaque((sb + b_slot(0) + wl0 + (g >> 1) * 2048) ^ ((2 * wn + t) << 5));
    xp = opaque(sb + b_slot(0) + 16384 + wm * 8192 + lane * 16);
    auto storeA = [&](int i) {
      __builtin_amdgcn_raw_buffer_store_b128(hA[i], g2.sx, lane * 16, hoffA + (i & 3) * (144 * 1024) + (i >> 2) * 1024, 16);
    };
    auto conv1 = [&](int cq) {
      const int gi = cq >> 2, pr = gi >> 2, mb = gi & 3, cT = 2 * pr + ((cq >> 1) & 1), ch = cq & 1;
      hA[gi][cq & 3] = h_dword(accA[0][cT][mb][2 * ch], accA[0][cT][mb][2 * ch + 1], accA[1][cT][mb][2 * ch], accA[1][cT][mb][2 * ch + 1]);
    };
    wait_vm<6>();
    stage_barrier();
#pragma unroll
    for (int mb = 0; mb < 4; ++mb) xf[0][mb] = ld_frag(xp, mb * 2048);
    wf[0] = tr_frag(wp[0], 0, 4096);
    wf[1] = tr_frag(wp[0], 1024, 1024 + 4096);
    dmaB(2, 0); dmaB(2, 1);
    mfma_drain();
    FSTAMP(19);
    SCHED0();
    static_for<9 * NU>([&](auto UC) {
      constexpr int U = decltype(UC)::value, st = U / NU, u = U % NU;
      constexpr int T = (u >> 1) & 1, gu = u & 1;
      constexpr bool trans = (u == NU - 2 && st + 1 < 9);
      constexpr bool rd = (u + 2 < NU) || (st + 1 < 9);
      constexpr int un = (u + 2) % NU;
      constexpr int wo = (un >> 2) * 8192 + (un & 1) * 1024;
      auto mm = [&](int mb) {
        if constexpr (st == 0 && u < 4) mfma16_first(acc[gu][T][mb], wf[U % 3], xf[u >> 2][mb]);
        else mfma16(acc[gu][T][mb], wf[U % 3], xf[u >> 2][mb]);
        SCHED0();
      };
      v4s lo, hi;
      mm(0);
      if constexpr (!trans && rd) { lo = tr_half(wp[(un >> 1) & 1], wo); }
      if constexpr (U < 16) conv1(2 * U);
      SCHED0();
      mm(1);
      if constexpr (trans) {
        wait_vm<gb_wait(st)>();
        stage_barrier();
        if constexpr (st == 4) h_signal(hsig, lane);
        if constexpr (st == 6) poll_issue(pollA, hflg);
        if constexpr (st == 7) { h_signal(hsig + 64 * 32, lane); flags_check(pollA, hflg); }
        FSTAMP(20 + st);
        constexpr int dl = b_slot(st + 1) - b_slot(st);
#pragma unroll
        for (int t = 0; t < 2; ++t) wp[t] = opaque(wp[t] + dl);
        xp = opaque(xp + dl);
        lo = tr_half(wp[(un >> 1) & 1], wo);
      }
      if constexpr (rd) { hi = tr_half(wp[(un >> 1) & 1], wo + 2 * 2048); wf[(U + 2) % 3] = join(lo, hi); }
      SCHED0();
      mm(2);
      if constexpr (u == 1 || u == 2) { xf[1][2 * (u - 1)] = ld_frag(xp, (2 * (u - 1)) * 2048 + 1024); xf[1][2 * (u - 1) + 1] = ld_frag(xp, (2 * (u - 1) + 1) * 2048 + 1024); }
      if constexpr (u >= NU - 2 && st + 1 < 9) { xf[0][2 * (u - 6)] = ld_frag(xp, (2 * (u - 6)) * 2048); xf[0][2 * (u - 6) + 1] = ld_frag(xp, (2 * (u - 6) + 1) * 2048); }
      if constexpr (U < 16) conv1(2 * U + 1);
      SCHED0();
      mm(3);
      if constexpr (u >= NU - 2) { if constexpr (st + 3 < 9) dmaB(st + 3, u - (NU - 2)); }
      else if constexpr (u < 4) { if constexpr (st + 2 < 9) dmaB(st + 2, u + 2); }
      if constexpr ((st == 1 || st == 2) && (u == 4 || u == 5)) storeA(2 * (st - 1) + u - 4);
      if constexpr (st >= 3 && st <= 6 && u == 4) storeA(st + 1);
      constexpr int qG = U - (7 * NU + 6);
      if constexpr (qG >= 0 && qG < 4) { g2_piece(g2, lane, (2 * qG) / 3, (2 * qG) % 3); g2_piece(g2, lane, (2 * qG + 1) / 3, (2 * qG + 1) % 3); }
      if constexpr (qG == 4) g2_piece(g2, lane, 2, 2);
      if constexpr (qG >= 5 && qG < 8) { g2_piece(g2, lane, qG - 5, 3); g2_piece(g2, lane, qG - 5, 4); }
      SCHED0();
    });
    mfma_drain();
    SCHED0();
    STAMP(ws, 1, 2);
    FSTAMP(28);
    FS_DUMP(ws, 29);
#pragma unroll
    for (int mb = 0; mb < 4; ++mb)
#pragma unroll
      for (int c = 0; c < 4; ++c)
        hB[mb][c] = h_dword(acc[0][c >> 1][mb][2 * (c & 1)], acc[0][c >> 1][mb][2 * (c & 1) + 1],
                            acc[1][c >> 1][mb][2 * (c & 1)], acc[1][c >> 1][mb][2 * (c & 1) + 1]);
  }
}

template <int NT, bool PRE>
DI void gemm2_body(char* smem, u32 sb, const G2Dma& g2, int w, int lane, int kg, int wm, int tbase, int tile, int ksl,
                   const u16* __restrict__ xin, u16* __restrict__ out, char* ws, u32* hsigB, const v4u (&hB)[4],
                   u32 hBoff) {
  constexpr int NU = NT;
  constexpr int NO = (2 * NT + 3) / 4;
  v4f acc[NT][4];
  STAMP(ws, 2, 0);
  FS_DECL
  FSTAMP(0);
  const int g = lane >> 4, qq = (lane & 15) >> 2, p = lane & 3;
  const int rr = 4 * (g & 1) + qq;
  const int wl0 = rr * 128 + (2 * (rr >> 1)) * 16 + p * 8;
  u32 wp[NT], xp;
#pragma unroll
  for (int t = 0; t < 4; ++t)
    wp[t] = opaque((sb + g_slot(0) + wl0 + ((g >> 1) + 4 * kg) * 2304 + (NT == 5 ? 1024 : 0)) ^ (t << 5));
  if constexpr (NT == 5) wp[4] = opaque(sb + g_slot(0) + 2048 + rr * 32 + p * 8 + ((g >> 1) + 4 * kg) * 2304);
  xp = opaque(sb + g_slot(0) + G_XOFF + wm * 8192 + kg * 1024 + lane * 16);
  auto rdW = [&](int u) -> v8s { return tr_frag(wp[u], 0, 2 * 2304); };
  const u32* hflg = (const u32*)(ws + WS_HCNT) + ((tile >> 2) * 4 + ksl) * 32 + (lane < 24 ? lane : 0);
  u32* ycnt = (u32*)(ws + WS_YCNT);
  v8s wf[3], xf[2][4];
  u32 pollB = 0, pollY = 0;
  const int j = 2 * wm + kg;
  const int tok0 = (tile >> 2) * 128 + j * 32 + (lane & 15) + 16 * (ksl & 1);
  const int dcol = (tile & 3) * 144 + tbase * 16 + (lane >> 4) * 4 + 16 * (ksl >> 1);
  v2u xv[NO]; u32 yv[NO][4]; v2f stt;
  constexpr int NL = 1 + 5 * NO;
  char* xw = smem + ((wm * 2 + kg) * 9 + tbase) * 2048 + lane * 16;
  if constexpr (!PRE) {
#pragma unroll
    for (int st = 0; st < 3; ++st)
#pragma unroll
      for (int i = 0; i < 3; ++i) g2_piece(g2, lane, st, i);
    wait_flags(hflg);
#pragma unroll
    for (int st = 0; st < 3; ++st) { g2_piece(g2, lane, st, 3); g2_piece(g2, lane, st, 4); }
  }
  wait_vm<4>();
  stage_barrier();
#pragma unroll
  for (int mb = 0; mb < 4; ++mb) xf[0][mb] = ld_frag(xp, mb * 2048);
  STAMP(ws, 2, 1);
  wf[0] = rdW(0);
  wf[1] = rdW(1);
  g2_piece(g2, lane, 3, 0); g2_piece(g2, lane, 3, 1);
  SCHED0();
  static_for<18 * NU>([&](auto UC) {
    constexpr int U = decltype(UC)::value, st = U / NU, u = U % NU;
    constexpr int SB = PRE ? 2 : 0;
    constexpr bool trans = (u == NU - 2 && st + 1 < 18);
    constexpr bool rd = (u + 2 < NU) || (st + 1 < 18);
    constexpr int un = (u + 2) % NU;
    auto mm = [&](int mb) {
      if constexpr (st == 0) mfma16_first(acc[u][mb], wf[U % 3], xf[st & 1][mb]);
      else mfma16(acc[u][mb], wf[U % 3], xf[st & 1][mb]);
      SCHED0();
    };
    v4s lo, hi;
    mm(0);
    if constexpr (!trans && rd) { lo = tr_half(wp[un], 0); SCHED0(); }
    mm(1);
    if constexpr (trans) {
      constexpr int NW = st == 0 ? 7 + SB : st == 1 ? 11 + 2 * SB : st == 2 ? 10 + SB : st == 3 ? 10 + SB
                         : st + 1 <= 15 ? 10 : st + 1 == 16 ? 5 : 0;
      wait_vm<NW>();
      stage_barrier();
      if constexpr (st == 0) poll_issue(pollB, hflg + 64 * 32);
      if constexpr (st == 2) flags_check(pollB, hflg + 64 * 32);
      if constexpr (PRE && st == 4) h_signal(hsigB, lane);
      if constexpr (st == 6) poll_issue(pollB, hflg + 128 * 32);
      if constexpr (st == 8) flags_check(pollB, hflg + 128 * 32);
      if constexpr (st == 12) poll_issue(pollY, ycnt);
      FSTAMP(1 + st);
      constexpr int dl = g_slot(st + 1) - g_slot(st);
#pragma unroll
      for (int t = 0; t < NT; ++t) wp[t] = opaque(wp[t] + dl);
      xp = opaque(xp + dl);
      lo = tr_half(wp[un], 0);
    }
    if constexpr (rd) { hi = tr_half(wp[un], 2 * 2304); wf[(U + 2) % 3] = join(lo, hi); }
    SCHED0();
    mm(2);
    if constexpr (u >= NU - 2 && st + 1 < 18) {
      xf[(st + 1) & 1][2 * (u - (NU - 2))] = ld_frag(xp, (2 * (u - (NU - 2))) * 2048);
      xf[(st + 1) & 1][2 * (u - (NU - 2)) + 1] = ld_frag(xp, (2 * (u - (NU - 2)) + 1) * 2048);
    }
    if constexpr (u == 0) { if constexpr (st + 3 < 18) g2_piece(g2, lane, st + 3, 2); }
    SCHED0();
    mm(3);
    if constexpr (u >= NU - 2) { if constexpr (st + 4 < 18) g2_piece(g2, lane, st + 4, u - (NU - 2)); }
    else if constexpr (u == 0) { if constexpr (st + 3 < 18) g2_piece(g2, lane, st + 3, 3); }
    else if constexpr (u == 1) { if constexpr (st + 3 < 18) g2_piece(g2, lane, st + 3, 4); }
    if constexpr (PRE && u == 1 && st < 2) {
#pragma unroll
      for (int mb = 2 * st; mb < 2 * st + 2; ++mb)
        __builtin_amdgcn_raw_buffer_store_b128(hB[mb], g2.sx, lane * 16, hBoff + mb * (144 * 1024), 16);
    }
    if constexpr (st == 17 && u >= 1) {
#pragma unroll
      for (int b2 = 0; b2 < 2; ++b2) *(v4f*)(xw + ((u - 1) * 2 + b2) * 1024) = kg ? acc[u - 1][b2] : acc[u - 1][2 + b2];
    }
    SCHED0();
  });
  STAMP(ws, 2, 2);
  FSTAMP(18);
  mfma_drain();
  SCHED0();
  FSTAMP(19);
#pragma unroll
  for (int b2 = 0; b2 < 2; ++b2) *(v4f*)(xw + ((NT - 1) * 2 + b2) * 1024) = kg ? acc[NT - 1][b2] : acc[NT - 1][2 + b2];
  stage_barrier();
  v4f r[2 * NT];
  {
    const char* rd = smem + ((wm * 2 + (kg ^ 1)) * 9 + tbase) * 2048 + lane * 16;
#pragma unroll
    for (int T = 0; T < NT; ++T)
#pragma unroll
      for (int b2 = 0; b2 < 2; ++b2) {
        const v4f o = *(const v4f*)(rd + (T * 2 + b2) * 1024);
        r[T * 2 + b2] = kg ? (o + acc[T][2 + b2]) : (acc[T][b2] + o);
      }
  }
  FSTAMP(20);
  const srd_t sp = make_srd(ws + WS_PART, 64 * 4 * 8 * 4 * 3072);
#pragma unroll
  for (int f = 0; f < 2 * NT; ++f)
    if ((f & 3) != ksl)
      __builtin_amdgcn_raw_buffer_store_b128(__builtin_bit_cast(v4u, r[f]), sp, lane * 16,
                                             (u32)((((tile * 4 + (f & 3)) * 8 + w) * 4 + ksl) * 3072 + (f >> 2) * 1024), 16);
  v4f own[NO];
#pragma unroll
  for (int i = 0; i < NO; ++i) {
    const v4f a0 = r[4 * i], a1 = r[4 * i + 1];
    const v4f a2 = (4 * i + 2 < 2 * NT) ? r[(4 * i + 2 < 2 * NT) ? 4 * i + 2 : 0] : a0;
    const v4f a3 = (4 * i + 3 < 2 * NT) ? r[(4 * i + 3 < 2 * NT) ? 4 * i + 3 : 0] : a0;
    own[i] = ksl == 0 ? a0 : ksl == 1 ? a1 : ksl == 2 ? a2 : a3;
  }
  FSTAMP(21);
  SCHED0();
  poll_check(pollY, ycnt, 576u);
  {
    const srd_t sy = make_srd(ws + WS_YT, S_ * D_ * 2);
    stt = ((const v2f*)(ws + WS_STATS))[tok0];
#pragma unroll
    for (int i = 0; i < NO; ++i) {
      const int dc = (4 * i + ksl < 2 * NT) ? dcol + 32 * i : dcol;
      xv[i] = *(const v2u*)(xin + (size_t)tok0 * D_ + dc);
#pragma unroll
      for (int r = 0; r < 4; ++r) yv[i][r] = __builtin_amdgcn_raw_buffer_load_b16(sy, (u32)(((dc + r) * S_ + tok0) * 2), 0, 16);
    }
  }
  SCHED0();
  wait_vm<NL>();
  FSTAMP(22);
  u32* flags = (u32*)(ws + WS_CNT);
  if (lane < 4 && lane != ksl)
    __hip_atomic_store(flags + ((tile * 4 + lane) * 8 + w) * 16 + ksl, 1u, __ATOMIC_RELAXED, __HIP_MEMORY_SCOPE_AGENT);
  STAMP(ws, 2, 3);
  float xa[NO][4];
#pragma unroll
  for (int i = 0; i < NO; ++i)
#pragma unroll
    for (int e = 0; e < 4; ++e) {
      const float xf_ = (e & 1) ? bf_hi(xv[i][e >> 1]) : bf_lo(xv[i][e >> 1]);
      const float xn = rbf(rbf(xf_ - stt[0]) * stt[1]);
      xa[i][e] = rbf(xf_ + rbf(xn * bf_lo(yv[i][e])));
    }
  {
    u32* fl = flags + ((tile * 4 + ksl) * 8 + w) * 16;
    u32 spins = 0;
    for (;;) {
      const u32 v = (lane < 4 && lane != ksl) ? __hip_atomic_load(fl + lane, __ATOMIC_RELAXED, __HIP_MEMORY_SCOPE_AGENT) : 1u;
      if (__all(v != 0u)) break;
      __builtin_amdgcn_s_sleep(1);
      if (++spins > (1u << 24)) break;
    }
  }
  __builtin_amdgcn_fence(__ATOMIC_ACQUIRE, "wavefront");
  STAMP(ws, 2, 4);
  FSTAMP(23);
  v4f pp[3][NO];
#pragma unroll
  for (int i3 = 0; i3 < 3; ++i3) {
    const int src = i3 + (i3 >= ksl ? 1 : 0);
    const u32 po = (u32)((((tile * 4 + ksl) * 8 + w) * 4 + src) * 3072);
#pragma unroll
    for (int i = 0; i < NO; ++i)
      pp[i3][i] = __builtin_bit_cast(v4f, __builtin_amdgcn_raw_buffer_load_b128(sp, lane * 16, po + i * 1024, 16));
  }
#pragma unroll
  for (int i = 0; i < NO; ++i) {
    const v4f s0 = ksl == 0 ? own[i] : pp[0][i];
    const v4f s1 = ksl == 1 ? own[i] : (ksl < 1 ? pp[0][i] : pp[1][i]);
    const v4f s2 = ksl == 2 ? own[i] : (ksl < 2 ? pp[1][i] : pp[2][i]);
    const v4f s3 = ksl == 3 ? own[i] : pp[2][i];
    const v4f B = (s0 + s1) + (s2 + s3);
    const float o0 = xa[i][0] + rbf(B[0]), o1 = xa[i][1] + rbf(B[1]);
    const float o2 = xa[i][2] + rbf(B[2]), o3 = xa[i][3] + rbf(B[3]);
    if (4 * i + ksl < 2 * NT)
      *(v2u*)(out + (size_t)tok0 * D_ + dcol + 32 * i) = v2u{pack_bf(o0, o1), pack_bf(o2, o3)};
  }
  STAMP(ws, 2, 5);
  FSTAMP(24);
  FS_DUMP2(ws, 25);
}

__global__ __launch_bounds__(512, 2) void fused_kernel(const u16* __restrict__ Wg, const u16* __restrict__ Wu,
                                                       const u16* __restrict__ Wd, const u16* __restrict__ ssmk,
                                                       const u16* __restrict__ ssmD, const u16* __restrict__ xin,
                                                       u16* __restrict__ out, char* __restrict__ ws) {
  extern __shared__ __attribute__((aligned(1024))) char smem[];
  const int bid = blockIdx.x;
  const int xcd = bid & 7, idx = bid >> 3;
  const int tid = threadIdx.x, lane = tid & 63;
  const int w = __builtin_amdgcn_readfirstlane(tid >> 6);
  const int ksl = xcd >> 1;
  const bool isconv = idx >= 24;
  const int tile2 = isconv ? (2 * (idx - 24) + (xcd & 1)) * 4 + 3 : (2 * (idx / 3) + (xcd & 1)) * 4 + idx % 3;
  const u32 sb = lds_addr(smem);
  G2Dma g2;
  g2.sw = make_srd(Wd, D_ * F_ * 2);
  g2.sx = make_srd(ws + WS_HT, S_ * F_ * 2);
  g2.wsoffA = (u32)((ksl * 1152 + 8 * (w & 3) + (w >> 2) * 64) * (D_ * 2) + (tile2 & 3) * 288);
  g2.wsoffB = (u32)((ksl * 1152 + 8 * w) * (D_ * 2) + (tile2 & 3) * 288);
  g2.xsoff = (u32)((((tile2 >> 2) * 8 + w) * 144 + ksl * 36) * 1024);
  g2.m0w = sb + w * 2304;
  g2.m0x = sb + G_XOFF + w * 2048;
  g2.woff = (u32)((lane >> 3) * (D_ * 2) + ((lane & 7) ^ (2 * (lane >> 4))) * 16);
  g2.soff = (u32)(((lane >> 1) & 7) * (D_ * 2) + (lane & 1) * 16);
  g2.xoff = lane * 16;
  const int kg2 = w >> 2, wm2 = (w >> 1) & 1, wn2 = (w & 1) ^ kg2;
  v4u hB[4];
  if (isconv) {
    const int cb = xcd * 8 + (idx - 24);
    STAMP(ws, 3, 0);
    if (w == 7) {
      const int c8 = idx - 24;
      u32 t[42];
#pragma unroll
      for (int i = 0; i < 14; ++i) {
        int e = (i * 8 + c8) * 64 + lane;
        e = e < 6912 ? e : 0;
        const int row = e / 12, r12 = e % 12, mtx = r12 / 6, l6 = r12 % 6;
        t[i] = *(const u32*)((const char*)(mtx ? Wu : Wg) + (size_t)row * (F_ * 2) + xcd * 1152 + (l6 >> 1) * 384 + (l6 & 1) * 128);
      }
#pragma unroll
      for (int i = 0; i < 7; ++i) {
        int e = (i * 8 + c8) * 64 + lane;
        e = e < 3456 ? e : 0;
        const int row = e / 6, r6 = e % 6, mtx = r6 / 3, l3 = r6 % 3;
        t[14 + i] = *(const u32*)((const char*)(mtx ? Wu : Wg) + (size_t)row * (F_ * 2) + xcd * 1152 + l3 * 384 + 256);
      }
#pragma unroll
      for (int i = 0; i < 21; ++i) {
        int e = (i * 8 + c8) * 64 + lane;
        e = e < 10368 ? e : 0;
        t[21 + i] = *(const u32*)((const char*)Wd + (size_t)ksl * (1152 * 1152) + (size_t)e * 128);
      }
      u32 a = 0;
#pragma unroll
      for (int i = 0; i < 42; ++i) a ^= t[i];
      asm volatile("" :: "v"(a));
    }
#pragma unroll 1
    for (int c = w; c < 9 && w < 7; c += 7) {
      conv_channel(smem + w * C_WAVE, cb * 9 + c, lane, ssmk, ssmD, ws);
      STAMP(ws, 3, 1 + c / 7);
    }
    stage_barrier();
#pragma unroll
    for (int i = 0; i < 4; ++i) hB[i] = v4u{0u, 0u, 0u, 0u};
    if (wn2 == 0) gemm2_body<5, false>(smem, sb, g2, w, lane, kg2, wm2, 4, tile2, ksl, xin, out, ws, nullptr, hB, 0u);
    else          gemm2_body<4, false>(smem, sb, g2, w, lane, kg2, wm2, 0, tile2, ksl, xin, out, ws, nullptr, hB, 0u);
  } else {
    const int tile_n = xcd * 3 + idx % 3, tile_m = idx / 3;
    const int wm = w & 3, wn = w >> 2;
    const srd_t sw = make_srd((w >> 2) ? Wu : Wg, D_ * F_ * 2);
    const srd_t sx = make_srd(ws + WS_XNT, S_ * D_ * 2);
    const u32 wsoff = (u32)((8 * (w & 3)) * (F_ * 2) + tile_n * 384);
    const u32 xsoff = (u32)((tile_m * 16 + 2 * w) * (18 * 1024));
    u32* hsigA = (u32*)(ws + WS_HCNT) + ((2 * tile_m + (wm >> 1)) * 4 + ksl) * 32 + (tile_n % 6) * 4 + (wm & 1) * 2 + wn;
    const u32* hflg = (const u32*)(ws + WS_HCNT) + ((tile2 >> 2) * 4 + ksl) * 32 + (lane < 24 ? lane : 0);
    const u32 hoff = (u32)(((tile_m * 16 + wm * 4) * 144 + tile_n * 6) * 1024);
    gemm1_body(sb, sw, sx, wsoff, xsoff, w, lane, wm, wn, ws, hoff + 2 * wn * 1024, hsigA, g2, hflg, hB);
    const u32 hBoff = hoff + (4 + wn) * 1024;
    if (wn2 == 0) gemm2_body<5, true>(smem, sb, g2, w, lane, kg2, wm2, 4, tile2, ksl, xin, out, ws, hsigA + 128 * 32, hB, hBoff);
    else          gemm2_body<4, true>(smem, sb, g2, w, lane, kg2, wm2, 0, tile2, ksl, xin, out, ws, hsigA + 128 * 32, hB, hBoff);
  }
}

extern "C" void kernel_launch(void* const* d_in, const int* in_sizes, int n_in, void* d_out, int out_size,
                              void* d_ws, size_t ws_size, hipStream_t stream) {
  const u16* q = (const u16*)d_in[0];
  const u16* k = (const u16*)d_in[1];
  const u16* v = (const u16*)d_in[2];
  const u16* shk = (const u16*)d_in[3];
  const u16* ssmk = (const u16*)d_in[4];
  const u16* ssmD = (const u16*)d_in[5];
  const u16* Wg = (const u16*)d_in[7];
  const u16* Wu = (const u16*)d_in[8];
  const u16* Wd = (const u16*)d_in[9];
  char* ws = (char*)d_ws;
  (void)hipFuncSetAttribute((const void*)fused_kernel, hipFuncAttributeMaxDynamicSharedMemorySize, H_LDS);
  pre_kernel<<<256 + 288, 256, 0, stream>>>(q, k, v, shk, ws);
  fused_kernel<<<256, 512, H_LDS, stream>>>(Wg, Wu, Wd, ssmk, ssmD, q, (u16*)d_out, ws);
}
```

```cpp
#include <hip/hip_runtime.h>
#include <hip/hip_bf16.h>
#include <stdint.h>
#include <utility>

typedef unsigned short u16;
typedef unsigned int u32;
typedef short v4s __attribute__((ext_vector_type(4)));
typedef short v8s __attribute__((ext_vector_type(8)));
typedef float v4f __attribute__((ext_vector_type(4)));
typedef float v2f __attribute__((ext_vector_type(2)));
typedef u32 v4u __attribute__((ext_vector_type(4)));
typedef u32 v2u __attribute__((ext_vector_type(2)));
typedef __bf16 v2bf __attribute__((ext_vector_type(2)));
typedef __attribute__((address_space(3))) v4s* lds_v4s_ptr;

constexpr int S_ = 2048, D_ = 576, F_ = 4608;

#define DI __device__ __forceinline__

template <int... I, class Fn>
DI void static_for_impl(std::integer_sequence<int, I...>, Fn&& f) { (f(std::integral_constant<int, I>{}), ...); }
template <int N, class Fn>
DI void static_for(Fn&& f) { static_for_impl(std::make_integer_sequence<int, N>{}, f); }

DI float bf_lo(u32 w) { return __uint_as_float(w << 16); }
DI float bf_hi(u32 w) { return __uint_as_float(w & 0xffff0000u); }
DI float rbf(float f) { return (float)(__bf16)f; }
DI u32 pack_bf(float lo, float hi) {
  v2f t = {lo, hi};
  v2bf b = __builtin_convertvector(t, v2bf);
  return __builtin_bit_cast(u32, b);
}

template <int CTRL> DI float dpp_f(float v) {
  return __builtin_bit_cast(float, __builtin_amdgcn_update_dpp(0, __builtin_bit_cast(int, v), CTRL, 0xf, 0xf, false));
}
DI float sum32(float v) {
  v += dpp_f<0x128>(v);
  v += dpp_f<0x124>(v);
  v += dpp_f<0x122>(v);
  v += dpp_f<0x121>(v);
  return v + __shfl_xor(v, 16);
}
constexpr size_t WS_XNT = 0;
constexpr size_t WS_STATS = WS_XNT + (size_t)S_ * D_ * 2;
constexpr size_t WS_KVT = WS_STATS + (size_t)S_ * 8;
constexpr size_t WS_YT = WS_KVT + (size_t)S_ * D_ * 2;
constexpr size_t WS_HT = WS_YT + (size_t)S_ * D_ * 2;
constexpr size_t WS_PART = WS_HT + (size_t)S_ * F_ * 2;
constexpr size_t WS_CNT = WS_PART + (size_t)64 * 4 * 8 * 4 * 3072;
constexpr size_t WS_HCNT = WS_CNT + 2048 * 64;
constexpr size_t WS_YCNT = WS_HCNT + 3 * 64 * 128;

__global__ __launch_bounds__(256) void pre_kernel(const u16* __restrict__ x, const u16* __restrict__ kin,
                                                  const u16* __restrict__ vin, const u16* __restrict__ shk,
                                                  char* __restrict__ ws) {
  const int lane = threadIdx.x & 63, w = threadIdx.x >> 6;
  if (blockIdx.x < 8) *(v4u*)(ws + WS_CNT + (size_t)(blockIdx.x * 256 + threadIdx.x) * 64) = v4u{0u, 0u, 0u, 0u};
  else if (blockIdx.x < 15) *(v4u*)(ws + WS_HCNT + (size_t)((blockIdx.x - 8) * 256 + threadIdx.x) * 16) = v4u{0u, 0u, 0u, 0u};
  if (blockIdx.x < 256) {
    const int c = lane & 31;
    const int row = blockIdx.x * 8 + w * 2 + (lane >> 5);
    const v4u* src = (const v4u*)(x + (size_t)row * D_);
    v4u d[3];
    d[0] = src[c]; d[1] = src[c + 32]; d[2] = src[64 + (c & 7)];
    const float lastw = (c < 8) ? 1.f : 0.f;
    float f[3][8];
#pragma unroll
    for (int i = 0; i < 3; ++i)
#pragma unroll
      for (int j = 0; j < 4; ++j) { f[i][2 * j] = bf_lo(d[i][j]); f[i][2 * j + 1] = bf_hi(d[i][j]); }
    float s0 = 0.f, s1 = 0.f, s2 = 0.f;
#pragma unroll
    for (int j = 0; j < 8; ++j) { s0 += f[0][j]; s1 += f[1][j]; s2 += f[2][j]; }
    const float mean = sum32(s0 + s1 + s2 * lastw) * (1.0f / D_);
    float q0 = 0.f, q1 = 0.f, q2 = 0.f;
#pragma unroll
    for (int j = 0; j < 8; ++j) {
      const float a = f[0][j] - mean, b = f[1][j] - mean, e = f[2][j] - mean;
      q0 += a * a; q1 += b * b; q2 += e * e;
    }
    const float var = sum32(q0 + q1 + q2 * lastw) * (1.0f / D_);
    const float mb = rbf(mean);
    const float sd = rbf(sqrtf(rbf(rbf(var) + 1e-5f)));
    const float rinv = 1.0f / sd;
    if (c == 0) ((v2f*)(ws + WS_STATS))[row] = v2f{mb, rinv};
    char* xnt = ws + WS_XNT;
    const int tb = row >> 4, m = row & 15;
    const int cc = c & 3, T = cc >> 1, q0i = 2 * (cc & 1);
#pragma unroll
    for (int i = 0; i < 3; ++i) {
      u32 o[4];
#pragma unroll
      for (int j = 0; j < 4; ++j) {
        const u32 dd = pack_bf(f[i][2 * j] - mb, f[i][2 * j + 1] - mb);
        o[j] = pack_bf(bf_lo(dd) * rinv, bf_hi(dd) * rinv);
      }
      const int kb = 8 * i + (c >> 2);
      char* base = xnt + ((size_t)(tb * 18 + kb) * 64 + (q0i * 16 + m)) * 16 + 8 * T;
      if (i < 2 || c < 8) {
        *(v2u*)(base) = v2u{o[0], o[1]};
        *(v2u*)(base + 256) = v2u{o[2], o[3]};
      }
    }
  } else {
    const int b = blockIdx.x - 256;
    const int tt = b / 9, ct = b % 9;
    const int tg = lane >> 5, cx = lane & 31;
    const int s0 = tt * 64 + w * 16 + tg * 8, d0 = ct * 64 + cx * 2;
    u32 kk[11], vv[8];
#pragma unroll
    for (int i = 0; i < 11; ++i) {
      const int s = s0 - 3 + i;
      const u32 t = *(const u32*)(kin + (size_t)(s < 0 ? 0 : s) * D_ + d0);
      kk[i] = (i < 3 && s < 0) ? 0u : t;
    }
#pragma unroll
    for (int i = 0; i < 8; ++i) vv[i] = *(const u32*)(vin + (size_t)(s0 + i) * D_ + d0);
    const v4u w0 = *(const v4u*)(shk + (size_t)d0 * 4);
    u16* kvt = (u16*)(ws + WS_KVT);
#pragma unroll
    for (int ch = 0; ch < 2; ++ch) {
      const u32 wa = w0[2 * ch], wb = w0[2 * ch + 1];
      const float t0 = bf_lo(wa), t1 = bf_hi(wa), t2 = bf_lo(wb), t3 = bf_hi(wb);
      float kf[11];
#pragma unroll
      for (int i = 0; i < 11; ++i) kf[i] = ch ? bf_hi(kk[i]) : bf_lo(kk[i]);
      float r[8];
#pragma unroll
      for (int i = 0; i < 8; i += 2) {
        u32 a = pack_bf(kf[i] * t3, kf[i + 1] * t3);
        u32 p = pack_bf(kf[i + 1] * t2, kf[i + 2] * t2);
        a = pack_bf(bf_lo(a) + bf_lo(p), bf_hi(a) + bf_hi(p));
        p = pack_bf(kf[i + 2] * t1, kf[i + 3] * t1);
        a = pack_bf(bf_lo(a) + bf_lo(p), bf_hi(a) + bf_hi(p));
        p = pack_bf(kf[i + 3] * t0, kf[i + 4] * t0);
        a = pack_bf(bf_lo(a) + bf_lo(p), bf_hi(a) + bf_hi(p));
        r[i] = bf_lo(a) * (ch ? bf_hi(vv[i]) : bf_lo(vv[i]));
        r[i + 1] = bf_hi(a) * (ch ? bf_hi(vv[i + 1]) : bf_lo(vv[i + 1]));
      }
      v4u o = {pack_bf(r[7], r[6]), pack_bf(r[5], r[4]), pack_bf(r[3], r[2]), pack_bf(r[1], r[0])};
      *(v4u*)(kvt + (size_t)(d0 + ch) * S_ + s0) = o;
    }
  }
}

constexpr int G_STAGE = 34816, G_NBUF = 4, G_LDS = G_STAGE * G_NBUF;
constexpr int G_XOFF = 18432;

typedef __attribute__((address_space(3))) v8s* lds_v8s_ptr;
typedef __attribute__((address_space(3))) char* lds_ptr;
DI v8s tr_frag(u32 p, int off_lo, int off_hi) {
  v4s a = __builtin_amdgcn_ds_read_tr16_b64_v4i16((lds_v4s_ptr)((lds_ptr)p + off_lo));
  v4s b = __builtin_amdgcn_ds_read_tr16_b64_v4i16((lds_v4s_ptr)((lds_ptr)p + off_hi));
  return __builtin_shufflevector(a, b, 0, 1, 2, 3, 4, 5, 6, 7);
}
DI v4s tr_half(u32 p, int off) { return __builtin_amdgcn_ds_read_tr16_b64_v4i16((lds_v4s_ptr)((lds_ptr)p + off)); }
DI v8s join(const v4s& a, const v4s& b) { return __builtin_shufflevector(a, b, 0, 1, 2, 3, 4, 5, 6, 7); }
DI v8s ld_frag(u32 p, int off) { return *(lds_v8s_ptr)((lds_ptr)p + off); }
DI u32 opaque(u32 v) { asm volatile("" : "+v"(v)); return v; }
DI u32 lds_addr(const void* p) { return (u32)(size_t)(lds_ptr)p; }
typedef __amdgpu_buffer_rsrc_t srd_t;
DI srd_t make_srd(const void* p, u32 bytes) {
  return __builtin_amdgcn_make_buffer_rsrc((void*)p, 0, bytes, 0x00020000);
}
DI void dma16(u32 m0v, u32 voff, srd_t srd, u32 soff) {
  asm volatile("s_mov_b32 m0, %0\n\ts_nop 0\n\tbuffer_load_dwordx4 %1, %2, %3 offen lds"
               :: "s"(m0v), "v"(voff), "s"(srd), "s"(soff) : "memory");
}
DI void dma16_sc1(u32 m0v, u32 voff, srd_t srd, u32 soff) {
  asm volatile("s_mov_b32 m0, %0\n\ts_nop 0\n\tbuffer_load_dwordx4 %1, %2, %3 offen sc1 lds"
               :: "s"(m0v), "v"(voff), "s"(srd), "s"(soff) : "memory");
}
DI void wait_count(u32* p, u32 target) {
  u32 spins = 0;
  while (__hip_atomic_load(p, __ATOMIC_RELAXED, __HIP_MEMORY_SCOPE_AGENT) < target) {
    __builtin_amdgcn_s_sleep(1);
    if (++spins > (1u << 24)) break;
  }
  __builtin_amdgcn_fence(__ATOMIC_ACQUIRE, "wavefront");
}
#define SCHED0() __builtin_amdgcn_sched_barrier(0)
DI void mfma16(v4f& c, const v8s& a, const v8s& b) {
  asm volatile("v_mfma_f32_16x16x32_bf16 %0, %1, %2, %0" : "+v"(c) : "v"(a), "v"(b));
}
DI void mfma16_first(v4f& c, const v8s& a, const v8s& b) {
  asm volatile("v_mfma_f32_16x16x32_bf16 %0, %1, %2, 0" : "=&v"(c) : "v"(a), "v"(b));
}
DI void mfma_drain() { asm volatile("s_nop 15\n\ts_nop 15" ::: "memory"); }
template <int N> DI void wait_vm() { asm volatile("s_waitcnt vmcnt(%0)" :: "n"(N) : "memory"); }
DI void stage_barrier() { asm volatile("s_waitcnt lgkmcnt(0)\n\ts_barrier" ::: "memory"); }

constexpr int C_KH = 1152 * 8, C_XBYTES = 8 * 768, C_WAVE = C_KH + C_XBYTES;
constexpr int C_SH = 8 * C_WAVE;
typedef u32 v3u __attribute__((ext_vector_type(3)));

DI v3u kern3(srd_t sk, int dw) {
  v3u t;
  t[0] = __builtin_amdgcn_raw_buffer_load_b32(sk, (u32)(dw * 4), 0, 0);
  asm volatile("" ::: "memory");
  t[1] = __builtin_amdgcn_raw_buffer_load_b32(sk, (u32)(dw * 4 + 4), 0, 0);
  asm volatile("" ::: "memory");
  t[2] = __builtin_amdgcn_raw_buffer_load_b32(sk, (u32)(dw * 4 + 8), 0, 0);
  return t;
}
DI v4u units2(const v3u& d) {
  return v4u{d[0], d[1], __builtin_amdgcn_alignbit(d[1], d[0], 16), __builtin_amdgcn_alignbit(d[2], d[1], 16)};
}
DI void y_store(srd_t sy, const char* xl, const v4f& a, int n, int ib, int i16, int q, float Dd) {
  const int beta = 16 * n + i16;
  const v2u kvp = *(const v2u*)(xl + (2 * ib + (q >> 1)) * 768 + (beta + 16) * 16 + 8 * (1 - (q & 1)));
  const float k3 = bf_lo(kvp[0]), k2 = bf_hi(kvp[0]), k1 = bf_lo(kvp[1]), k0 = bf_hi(kvp[1]);
  const float y0 = rbf(a[0]) + rbf(k0 * Dd), y1 = rbf(a[1]) + rbf(k1 * Dd);
  const float y2 = rbf(a[2]) + rbf(k2 * Dd), y3 = rbf(a[3]) + rbf(k3 * Dd);
  __builtin_amdgcn_raw_buffer_store_b64(v2u{pack_bf(y0, y1), pack_bf(y2, y3)}, sy, (u32)((64 * beta + 16 * ib + 4 * q) * 2), 0, 16);
}

DI void conv_wg(char* smem, int cb, int w, int lane, int xcd, int c8, const u16* __restrict__ ssmk,
                const u16* __restrict__ ssmD, const u16* __restrict__ Wg, const u16* __restrict__ Wu,
                const u16* __restrict__ Wd, char* __restrict__ ws) {
  const int ch = cb * 9 + w, ch9 = cb * 9 + 8;
  char* kl = smem + w * C_WAVE;
  char* xl = kl + C_KH;
  char* kl9 = smem + C_SH;
  char* xl9 = kl9 + 2080 * 8;
  const srd_t sk = make_srd(ssmk + (size_t)ch * S_, S_ * 2);
  const srd_t sk9 = make_srd(ssmk + (size_t)ch9 * S_, S_ * 2);
  const v4u* xs = (const v4u*)(ws + WS_KVT + (size_t)ch * S_ * 2);
  const u32 dw0 = ((const u32*)ssmD)[ch >> 1], dw9 = ((const u32*)ssmD)[ch9 >> 1];
  const float Dd0 = (ch & 1) ? bf_hi(dw0) : bf_lo(dw0), Dd9 = (ch9 & 1) ? bf_hi(dw9) : bf_lo(dw9);
  v4u xd[4];
#pragma unroll
  for (int it = 0; it < 4; ++it) xd[it] = xs[it * 64 + lane];
  v3u kd[9];
  kd[0] = kern3(sk, lane - 16);
#pragma unroll
  for (int it = 1; it < 9; ++it) kd[it] = __builtin_amdgcn_raw_buffer_load_b96(sk, (u32)((it * 64 + lane - 16) * 4), 0, 0);
  v3u k9[3];
#pragma unroll
  for (int it = 0; it < 3; ++it) k9[it] = kern3(sk9, w * 130 + it * 64 + lane - 16);
  const v4u x9 = ((const v4u*)(ws + WS_KVT + (size_t)ch9 * S_ * 2))[w * 32 + (lane & 31)];
  u32 pf[6];
  const int wv = c8 * 8 + w;
  auto warmA = [&]() {
#pragma unroll
    for (int i = 0; i < 2; ++i) {
      int e = (i * 64 + wv) * 64 + lane;
      e = e < 6912 ? e : 0;
      const int row = e / 12, r12 = e % 12, mtx = r12 / 6, l6 = r12 % 6;
      pf[i] = *(const u32*)((const char*)(mtx ? Wu : Wg) + (size_t)row * (F_ * 2) + xcd * 1152 + (l6 >> 1) * 384 + (l6 & 1) * 128);
    }
  };
  auto warmB = [&]() {
    {
      int e = wv * 64 + lane;
      e = e < 3456 ? e : 0;
      const int row = e / 6, r6 = e % 6, mtx = r6 / 3, l3 = r6 % 3;
      pf[2] = *(const u32*)((const char*)(mtx ? Wu : Wg) + (size_t)row * (F_ * 2) + xcd * 1152 + l3 * 384 + 256);
    }
#pragma unroll
    for (int i = 0; i < 3; ++i) {
      int e = (i * 64 + wv) * 64 + lane;
      e = e < 10368 ? e : 0;
      pf[3 + i] = *(const u32*)((const char*)Wd + (size_t)(xcd >> 1) * (1152 * 1152) + (size_t)e * 128);
    }
  };
  SCHED0();
  warmA();
  SCHED0();
#pragma unroll
  for (int it = 0; it < 2; ++it) {
    const int z = it * 64 + lane;
    *(v4u*)(xl + (z >> 4) * 768 + (z & 15) * 16) = v4u{0u, 0u, 0u, 0u};
  }
  if (lane < 16) *(v4u*)(xl9 + w * 768 + lane * 16) = v4u{0u, 0u, 0u, 0u};
#pragma unroll
  for (int it = 0; it < 4; ++it) {
    const int cidx = it * 64 + lane;
    *(v4u*)(xl + (cidx & 7) * 768 + ((cidx >> 3) + 16) * 16) = xd[it];
  }
#pragma unroll
  for (int it = 0; it < 9; ++it)
    *(v4u*)(kl + (it * 64 + lane) * 16) = units2(kd[it]);
#pragma unroll
  for (int it = 0; it < 3; ++it)
    if (it < 2 || lane < 2) *(v4u*)(kl9 + (w * 130 + it * 64 + lane) * 16) = units2(k9[it]);
  if (lane < 32) {
    const int cidx = w * 32 + lane;
    *(v4u*)(xl9 + (cidx & 7) * 768 + ((cidx >> 3) + 16) * 16) = x9;
  }
#pragma unroll
  for (int it = 0; it < 8; ++it) kd[it] = __builtin_amdgcn_raw_buffer_load_b96(sk, (u32)((512 + it * 64 + lane - 16) * 4), 0, 0);
  kd[8] = kern3(sk, 1024 + lane - 16);
  SCHED0();
  warmB();
  SCHED0();
  __builtin_amdgcn_s_waitcnt(0xC07F);
  __builtin_amdgcn_wave_barrier();

  const int i16 = lane & 15, q = lane >> 4;
  const u32 ap = opaque(lds_addr(kl) + (i16 - 8 * q - 7 + 32) * 8);
  const u32 bp = opaque(lds_addr(xl) + q * 768 + i16 * 16);
  v4f acc[4][2];
  v8s af[3][4], bfr[2][4];
  auto rdA = [&](int c, int j) -> v8s {
    const int delta = 64 * (c & 15) + 16 * j;
    v4s lo = *(volatile __attribute__((address_space(3))) v4s*)((lds_ptr)ap + 8 * delta);
    v4s hi = *(volatile __attribute__((address_space(3))) v4s*)((lds_ptr)ap + 8 * delta + 32);
    return __builtin_shufflevector(lo, hi, 0, 1, 2, 3, 4, 5, 6, 7);
  };
  auto rdB = [&](int c, int n, int kb) -> v8s { return ld_frag(bp, 4 * kb * 768 + 16 * (16 * n - c + 16)); };
#pragma unroll
  for (int j = 0; j < 4; ++j) af[0][j] = rdA(0, j);
#pragma unroll
  for (int j = 0; j < 4; ++j) bfr[0][j] = rdB(0, j >> 1, j & 1);
  SCHED0();
  static_for<32>([&](auto CC) {
    constexpr int c = decltype(CC)::value;
    if constexpr (c == 15) {
      __builtin_amdgcn_s_waitcnt(0xC07F);
#pragma unroll
      for (int it = 0; it < 9; ++it)
        *(v4u*)(kl + (it * 64 + lane) * 16) = units2(kd[it]);
      __builtin_amdgcn_s_waitcnt(0xC07F);
      __builtin_amdgcn_wave_barrier();
    }
    if constexpr (c + 1 < 32) {
#pragma unroll
      for (int j = 0; j < 4; ++j) af[(c + 1) % 3][j] = rdA(c + 1, j);
#pragma unroll
      for (int j = 0; j < 4; ++j)
        if (j >= 2 || c + 1 < 16) bfr[(c + 1) & 1][j] = rdB(c + 1, j >> 1, j & 1);
    }
    SCHED0();
#pragma unroll
    for (int n = 1; n >= 0; --n) {
      if (n == 0 && c >= 16) continue;
#pragma unroll
      for (int kb = 0; kb < 2; ++kb) {
#pragma unroll
        for (int ib = 0; ib < 4; ++ib) {
          const int j = ib - 2 * kb;
          if (c == 0 && j < 0) continue;
          const v8s& A = (j >= 0) ? af[c % 3][j >= 0 ? j : 0] : af[(c + 2) % 3][j < 0 ? j + 4 : 0];
          if (c == 0 && kb == 0) mfma16_first(acc[ib][n], A, bfr[c & 1][2 * n + kb]);
          else mfma16(acc[ib][n], A, bfr[c & 1][2 * n + kb]);
        }
      }
    }
    SCHED0();
  });
  mfma_drain();
  SCHED0();
  {
    const float Dd = Dd0;
    const srd_t sy = make_srd(ws + WS_YT + (size_t)ch * S_ * 2, S_ * 2);
#pragma unroll
    for (int n = 0; n < 2; ++n)
#pragma unroll
      for (int ib = 0; ib < 4; ++ib) y_store(sy, xl, acc[ib][n], n, ib, i16, q, Dd);
  }
  stage_barrier();
  {
    const int c0 = w < 6 ? 3 * w : w == 6 ? 20 : 26, c1 = w < 5 ? 3 * w + 3 : w == 5 ? 20 : w == 6 ? 26 : 32;
    const char* a9 = kl9 + (i16 - 8 * q - 7 + 32) * 8;
    const char* b9 = xl9 + q * 768 + (i16 + 16) * 16;
    v4f a2[4][2];
#pragma unroll
    for (int a = 0; a < 4; ++a) { a2[a][0] = v4f{0.f, 0.f, 0.f, 0.f}; a2[a][1] = v4f{0.f, 0.f, 0.f, 0.f}; }
#pragma unroll 1
    for (int c = c0; c < c1; ++c) {
      v8s win[6];
#pragma unroll
      for (int j = 0; j < 6; ++j) {
        const int delta = 64 * c + 16 * (j - 2);
        const int dd = delta < 0 ? 0 : delta;
        const v4s lo = *(const v4s*)(a9 + 8 * dd), hi = *(const v4s*)(a9 + 8 * dd + 32);
        win[j] = __builtin_shufflevector(lo, hi, 0, 1, 2, 3, 4, 5, 6, 7);
        if (j < 2 && c == 0) win[j] = v8s{0, 0, 0, 0, 0, 0, 0, 0};
      }
#pragma unroll
      for (int kb = 0; kb < 2; ++kb) {
        const v8s b1 = *(const v8s*)(b9 + 4 * kb * 768 + 16 * (16 - c));
#pragma unroll
        for (int ib = 0; ib < 4; ++ib)
          a2[ib][1] = __builtin_amdgcn_mfma_f32_16x16x32_bf16(win[2 + ib - 2 * kb], b1, a2[ib][1], 0, 0, 0);
      }
      if (c < 16) {
#pragma unroll
        for (int kb = 0; kb < 2; ++kb) {
          const v8s b0 = *(const v8s*)(b9 + 4 * kb * 768 - 16 * c);
#pragma unroll
          for (int ib = 0; ib < 4; ++ib)
            a2[ib][0] = __builtin_amdgcn_mfma_f32_16x16x32_bf16(win[2 + ib - 2 * kb], b0, a2[ib][0], 0, 0, 0);
        }
      }
    }
#pragma unroll
    for (int t = 0; t < 8; ++t) *(v4f*)(kl + t * 1024 + lane * 16) = a2[t & 3][t >> 2];
    stage_barrier();
    v4f sum = *(const v4f*)(smem + w * 1024 + lane * 16);
#pragma unroll
    for (int v = 1; v < 8; ++v) sum += *(const v4f*)(smem + v * C_WAVE + w * 1024 + lane * 16);
    const float Dd = Dd9;
    const srd_t sy = make_srd(ws + WS_YT + (size_t)ch9 * S_ * 2, S_ * 2);
    y_store(sy, xl9, sum, w >> 2, w & 3, i16, q, Dd);
  }
  u32 pa = 0;
#pragma unroll
  for (int i = 0; i < 6; ++i) pa ^= pf[i];
  asm volatile("" :: "v"(pa));
  asm volatile("s_waitcnt vmcnt(0)" ::: "memory");
  stage_barrier();
  if (w == 0 && lane == 0) __hip_atomic_fetch_add((u32*)(ws + WS_YCNT), 9u, __ATOMIC_RELAXED, __HIP_MEMORY_SCOPE_AGENT);
}

constexpr int H_LDS = 163840;
constexpr int A_STAGE = 32768;
constexpr int b_slot(int s) { return (s % 3) == 0 ? 65536 : (s % 3) == 1 ? 114688 : 0; }
constexpr int g_slot(int s) { return (s & 3) == 0 ? 49152 : (s & 3) == 1 ? 83968 : (s & 3) == 2 ? 118784 : 0; }
constexpr int gb_store(int st) { return st == 1 ? 2 : st == 2 ? 2 : (st >= 3 && st <= 6) ? 1 : 0; }
constexpr int gb_extra(int st) { return (st == 4 || st == 6) ? 1 : 0; }
constexpr int gb_wait(int st) {
  return st >= 7 ? 0 : (st > 0 ? gb_store(st - 1) + gb_extra(st - 1) : 0) + 6 + gb_store(st);
}

DI void h_signal(u32* p, int lane) {
  if (lane == 0) __hip_atomic_store(p, 1u, __ATOMIC_RELAXED, __HIP_MEMORY_SCOPE_AGENT);
}
DI void wait_flags(const u32* p) {
  u32 spins = 0;
  while (!__all(__hip_atomic_load(p, __ATOMIC_RELAXED, __HIP_MEMORY_SCOPE_AGENT) != 0u)) {
    __builtin_amdgcn_s_sleep(1);
    if (++spins > (1u << 24)) break;
  }
  __builtin_amdgcn_fence(__ATOMIC_ACQUIRE, "wavefront");
}
DI u32 h_dword(float g0, float g1, float u0, float u1) {
  const u32 g = pack_bf(g0, g1), u = pack_bf(u0, u1);
  return pack_bf(bf_lo(g) * bf_lo(u), bf_hi(g) * bf_hi(u));
}
DI void poll_issue(u32& v, const u32* p) {
  asm volatile("global_load_dword %0, %1, off sc1" : "=v"(v) : "v"(p) : "memory");
}
DI void poll_check(u32& v, u32* p, u32 target) {
  asm volatile("" : "+v"(v));
  if (__builtin_amdgcn_readfirstlane(v) < target) wait_count(p, target);
}

DI void flags_check(u32& v, const u32* p) {
  asm volatile("" : "+v"(v));
  if (!__all(v != 0u)) wait_flags(p);
}

struct G2Dma {
  srd_t sw, sx;
  u32 wsoffA, wsoffB, xsoff, m0w, m0x, woff, soff, xoff;
};
DI void g2_piece(const G2Dma& d, int lane, int st, int i) {
  const u32 bo = g_slot(st);
  const int kb = st < 6 ? 6 * st : st < 12 ? 6 * (st - 6) + 1 : 6 * (st - 12) + 4;
  const int kb2 = kb + (st < 12 ? 2 : 1);
  const u32 wo = (st < 12 ? d.wsoffA : d.wsoffB) + (u32)kb * (32 * D_ * 2);
  if (i == 0) dma16(d.m0w + bo, d.woff, d.sw, wo);
  else if (i == 1) dma16(d.m0w + bo + 1024, d.woff, d.sw, wo + 128);
  else if (i == 2) { if (lane < 16) dma16(d.m0w + bo + 2048, d.soff, d.sw, wo + 256); }
  else if (i == 3) dma16_sc1(d.m0x + bo, d.xoff, d.sx, d.xsoff + kb * 1024);
  else dma16_sc1(d.m0x + bo + 1024, d.xoff, d.sx, d.xsoff + kb2 * 1024);
}

DI void gemm1_body(u32 sb, srd_t sw, srd_t sx, u32 wsoff, u32 xsoff, int w, int lane, int wm, int wn,
                   char* ws, u32 hoffA, u32* hsig, const G2Dma& g2, const u32* hflg, v4u (&hB)[4]) {
  constexpr int NU = 8;
  const int g = lane >> 4, qq = (lane & 15) >> 2, p = lane & 3;
  const int rr = 4 * (g & 1) + qq;
  const int wl0 = rr * 128 + (2 * (rr >> 1)) * 16 + p * 8;
  const u32 woff = (u32)((lane >> 3) * (F_ * 2) + ((lane & 7) ^ (2 * (lane >> 4))) * 16);
  const u32 xoff = lane * 16;
  v8s wf[3], xf[2][4];
  const u32 m0wB = sb + (w & 3) * 2048 + (w >> 2) * 1024;
  const u32 m0xB = sb + 16384 + w * 4096;
  auto dmaB = [&](int st, int i) {
    const u32 bo = b_slot(st);
    if (i < 2) dma16(m0wB + bo + i * 8192, woff, sw, wsoff + (u32)st * (64 * F_ * 2) + 256 + i * (32 * F_ * 2));
    else dma16(m0xB + bo + (i - 2) * 1024, xoff, sx, xsoff + st * 2048 + ((i - 2) >> 1) * (18 * 1024) + ((i - 2) & 1) * 1024);
  };
  v4f accA[2][4][4];
  {
    u32 wp[4], xp;
#pragma unroll
    for (int t = 0; t < 4; ++t) wp[t] = opaque((sb + wl0 + (g >> 1) * 4096 + wn * 1024) ^ (t << 5));
    xp = opaque(sb + 16384 + wm * 4096 + lane * 16);
    const u32 m0w = sb + (w & 3) * 4096 + (w >> 2) * 2048;
    const u32 m0x = sb + 16384 + w * 2048;
    auto dma_piece = [&](int st, int i) {
      const u32 bo = (st & 3) * A_STAGE;
      if (i < 2) dma16(m0w + bo + i * 1024, woff, sw, wsoff + (u32)st * (32 * F_ * 2) + i * 128);
      else dma16(m0x + bo + (i - 2) * 1024, xoff, sx, xsoff + st * 1024 + (i - 2) * (18 * 1024));
    };
#pragma unroll
    for (int st = 0; st < 3; ++st)
#pragma unroll
      for (int i = 0; i < 4; ++i) dma_piece(st, i);
    wait_vm<8>();
    stage_barrier();
#pragma unroll
    for (int mb = 0; mb < 4; ++mb) xf[0][mb] = ld_frag(xp, mb * 1024);
    wf[0] = tr_frag(wp[0], 0, 8192);
    wf[1] = tr_frag(wp[0], 2048, 2048 + 8192);
    dma_piece(3, 0); dma_piece(3, 1);
    SCHED0();
    static_for<18 * NU>([&](auto UC) {
      constexpr int U = decltype(UC)::value, st = U / NU, u = U % NU;
      constexpr int T = u >> 1, gu = u & 1;
      constexpr bool trans = (u == NU - 2 && st + 1 < 18);
      constexpr bool rd = (u + 2 < NU) || (st + 1 < 18);
      constexpr int un = (u + 2) % NU;
      auto mm = [&](int mb) {
        if constexpr (st == 0) mfma16_first(accA[gu][T][mb], wf[U % 3], xf[st & 1][mb]);
        else mfma16(accA[gu][T][mb], wf[U % 3], xf[st & 1][mb]);
        SCHED0();
      };
      v4s lo, hi;
      mm(0);
      if constexpr (!trans && rd) { lo = tr_half(wp[un >> 1], (un & 1) * 2048); SCHED0(); }
      mm(1);
      if constexpr (trans) {
        if constexpr (st + 1 <= 15) wait_vm<8>(); else if constexpr (st + 1 == 16) wait_vm<4>(); else wait_vm<8>();
        stage_barrier();
        constexpr int dl = (((st + 1) & 3) == 0) ? -3 * A_STAGE : A_STAGE;
#pragma unroll
        for (int t = 0; t < 4; ++t) wp[t] = opaque(wp[t] + dl);
        xp = opaque(xp + dl);
        lo = tr_half(wp[un >> 1], (un & 1) * 2048);
      }
      if constexpr (rd) { hi = tr_half(wp[un >> 1], (un & 1) * 2048 + 8192); wf[(U + 2) % 3] = join(lo, hi); }
      SCHED0();
      mm(2);
      if constexpr (u >= NU - 2 && st + 1 < 18) {
        xf[(st + 1) & 1][2 * (u - 6)] = ld_frag(xp, (2 * (u - 6)) * 1024);
        xf[(st + 1) & 1][2 * (u - 6) + 1] = ld_frag(xp, (2 * (u - 6) + 1) * 1024);
      }
      SCHED0();
      mm(3);
      if constexpr (u >= NU - 2) { if constexpr (st + 4 < 18) dma_piece(st + 4, u - (NU - 2)); }
      else if constexpr (u < 2) { if constexpr (st + 3 < 18) dma_piece(st + 3, u + 2); }
      constexpr int qB = U - (15 * NU + 6);
      if constexpr (qB >= 0 && qB < 12) dmaB(qB / 6, qB % 6);
      SCHED0();
    });
  }
  {
    v4f acc[2][2][4];
    v4u hA[8];
    u32 wp[2], xp, pollA = 0;
#pragma unroll
    for (int t = 0; t < 2; ++t) wp[t] = opaque((sb + b_slot(0) + wl0 + (g >> 1) * 2048) ^ ((2 * wn + t) << 5));
    xp = opaque(sb + b_slot(0) + 16384 + wm * 8192 + lane * 16);
    auto storeA = [&](int i) {
      __builtin_amdgcn_raw_buffer_store_b128(hA[i], g2.sx, lane * 16, hoffA + (i & 3) * (144 * 1024) + (i >> 2) * 1024, 16);
    };
    auto conv1 = [&](int cq) {
      const int gi = cq >> 2, pr = gi >> 2, mb = gi & 3, cT = 2 * pr + ((cq >> 1) & 1), ch = cq & 1;
      hA[gi][cq & 3] = h_dword(accA[0][cT][mb][2 * ch], accA[0][cT][mb][2 * ch + 1], accA[1][cT][mb][2 * ch], accA[1][cT][mb][2 * ch + 1]);
    };
    wait_vm<6>();
    stage_barrier();
#pragma unroll
    for (int mb = 0; mb < 4; ++mb) xf[0][mb] = ld_frag(xp, mb * 2048);
    wf[0] = tr_frag(wp[0], 0, 4096);
    wf[1] = tr_frag(wp[0], 1024, 1024 + 4096);
    dmaB(2, 0); dmaB(2, 1);
    mfma_drain();
    SCHED0();
    static_for<9 * NU>([&](auto UC) {
      constexpr int U = decltype(UC)::value, st = U / NU, u = U % NU;
      constexpr int T = (u >> 1) & 1, gu = u & 1;
      constexpr bool trans = (u == NU - 2 && st + 1 < 9);
      constexpr bool rd = (u + 2 < NU) || (st + 1 < 9);
      constexpr int un = (u + 2) % NU;
      constexpr int wo = (un >> 2) * 8192 + (un & 1) * 1024;
      auto mm = [&](int mb) {
        if constexpr (st == 0 && u < 4) mfma16_first(acc[gu][T][mb], wf[U % 3], xf[u >> 2][mb]);
        else mfma16(acc[gu][T][mb], wf[U % 3], xf[u >> 2][mb]);
        SCHED0();
      };
      v4s lo, hi;
      mm(0);
      if constexpr (!trans && rd) { lo = tr_half(wp[(un >> 1) & 1], wo); }
      if constexpr (U < 16) conv1(2 * U);
      SCHED0();
      mm(1);
      if constexpr (trans) {
        wait_vm<gb_wait(st)>();
        stage_barrier();
        if constexpr (st == 4) h_signal(hsig, lane);
        if constexpr (st == 6) poll_issue(pollA, hflg);
        if constexpr (st == 7) { h_signal(hsig + 64 * 32, lane); flags_check(pollA, hflg); }
        constexpr int dl = b_slot(st + 1) - b_slot(st);
#pragma unroll
        for (int t = 0; t < 2; ++t) wp[t] = opaque(wp[t] + dl);
        xp = opaque(xp + dl);
        lo = tr_half(wp[(un >> 1) & 1], wo);
      }
      if constexpr (rd) { hi = tr_half(wp[(un >> 1) & 1], wo + 2 * 2048); wf[(U + 2) % 3] = join(lo, hi); }
      SCHED0();
      mm(2);
      if constexpr (u == 1 || u == 2) { xf[1][2 * (u - 1)] = ld_frag(xp, (2 * (u - 1)) * 2048 + 1024); xf[1][2 * (u - 1) + 1] = ld_frag(xp, (2 * (u - 1) + 1) * 2048 + 1024); }
      if constexpr (u >= NU - 2 && st + 1 < 9) { xf[0][2 * (u - 6)] = ld_frag(xp, (2 * (u - 6)) * 2048); xf[0][2 * (u - 6) + 1] = ld_frag(xp, (2 * (u - 6) + 1) * 2048); }
      if constexpr (U < 16) conv1(2 * U + 1);
      SCHED0();
      mm(3);
      if constexpr (u >= NU - 2) { if constexpr (st + 3 < 9) dmaB(st + 3, u - (NU - 2)); }
      else if constexpr (u < 4) { if constexpr (st + 2 < 9) dmaB(st + 2, u + 2); }
      if constexpr ((st == 1 || st == 2) && (u == 4 || u == 5)) storeA(2 * (st - 1) + u - 4);
      if constexpr (st >= 3 && st <= 6 && u == 4) storeA(st + 1);
      constexpr int qG = U - (7 * NU + 6);
      if constexpr (qG >= 0 && qG < 4) { g2_piece(g2, lane, (2 * qG) / 3, (2 * qG) % 3); g2_piece(g2, lane, (2 * qG + 1) / 3, (2 * qG + 1) % 3); }
      if constexpr (qG == 4) g2_piece(g2, lane, 2, 2);
      if constexpr (qG >= 5 && qG < 8) { g2_piece(g2, lane, qG - 5, 3); g2_piece(g2, lane, qG - 5, 4); }
      SCHED0();
    });
    mfma_drain();
    SCHED0();
#pragma unroll
    for (int mb = 0; mb < 4; ++mb)
#pragma unroll
      for (int c = 0; c < 4; ++c)
        hB[mb][c] = h_dword(acc[0][c >> 1][mb][2 * (c & 1)], acc[0][c >> 1][mb][2 * (c & 1) + 1],
                            acc[1][c >> 1][mb][2 * (c & 1)], acc[1][c >> 1][mb][2 * (c & 1) + 1]);
  }
}

template <int NT, bool PRE>
DI void gemm2_body(char* smem, u32 sb, const G2Dma& g2, int w, int lane, int kg, int wm, int tbase, int tile, int ksl,
                   const u16* __restrict__ xin, u16* __restrict__ out, char* ws, u32* hsigB, const v4u (&hB)[4],
                   u32 hBoff) {
  constexpr int NU = NT;
  constexpr int NO = (2 * NT + 3) / 4;
  v4f acc[NT][4];
  const int g = lane >> 4, qq = (lane & 15) >> 2, p = lane & 3;
  const int rr = 4 * (g & 1) + qq;
  const int wl0 = rr * 128 + (2 * (rr >> 1)) * 16 + p * 8;
  u32 wp[NT], xp;
#pragma unroll
  for (int t = 0; t < 4; ++t)
    wp[t] = opaque((sb + g_slot(0) + wl0 + ((g >> 1) + 4 * kg) * 2304 + (NT == 5 ? 1024 : 0)) ^ (t << 5));
  if constexpr (NT == 5) wp[4] = opaque(sb + g_slot(0) + 2048 + rr * 32 + p * 8 + ((g >> 1) + 4 * kg) * 2304);
  xp = opaque(sb + g_slot(0) + G_XOFF + wm * 8192 + kg * 1024 + lane * 16);
  auto rdW = [&](int u) -> v8s { return tr_frag(wp[u], 0, 2 * 2304); };
  const u32* hflg = (const u32*)(ws + WS_HCNT) + ((tile >> 2) * 4 + ksl) * 32 + (lane < 24 ? lane : 0);
  u32* ycnt = (u32*)(ws + WS_YCNT);
  v8s wf[3], xf[2][4];
  u32 pollB = 0, pollY = 0;
  const int j = 2 * wm + kg;
  const int tok0 = (tile >> 2) * 128 + j * 32 + (lane & 15) + 16 * (ksl & 1);
  const int dcol = (tile & 3) * 144 + tbase * 16 + (lane >> 4) * 4 + 16 * (ksl >> 1);
  v2u xv[NO]; u32 yv[NO][4]; v2f stt;
  constexpr int NL = 1 + 5 * NO;
  char* xw = smem + ((wm * 2 + kg) * 9 + tbase) * 2048 + lane * 16;
  if constexpr (!PRE) {
#pragma unroll
    for (int st = 0; st < 3; ++st)
#pragma unroll
      for (int i = 0; i < 3; ++i) g2_piece(g2, lane, st, i);
    wait_flags(hflg);
#pragma unroll
    for (int st = 0; st < 3; ++st) { g2_piece(g2, lane, st, 3); g2_piece(g2, lane, st, 4); }
  }
  wait_vm<4>();
  stage_barrier();
#pragma unroll
  for (int mb = 0; mb < 4; ++mb) xf[0][mb] = ld_frag(xp, mb * 2048);
  wf[0] = rdW(0);
  wf[1] = rdW(1);
  g2_piece(g2, lane, 3, 0); g2_piece(g2, lane, 3, 1);
  SCHED0();
  static_for<18 * NU>([&](auto UC) {
    constexpr int U = decltype(UC)::value, st = U / NU, u = U % NU;
    constexpr int SB = PRE ? 2 : 0;
    constexpr bool trans = (u == NU - 2 && st + 1 < 18);
    constexpr bool rd = (u + 2 < NU) || (st + 1 < 18);
    constexpr int un = (u + 2) % NU;
    auto mm = [&](int mb) {
      if constexpr (st == 0) mfma16_first(acc[u][mb], wf[U % 3], xf[st & 1][mb]);
      else mfma16(acc[u][mb], wf[U % 3], xf[st & 1][mb]);
      SCHED0();
    };
    v4s lo, hi;
    mm(0);
    if constexpr (!trans && rd) { lo = tr_half(wp[un], 0); SCHED0(); }
    mm(1);
    if constexpr (trans) {
      constexpr int NW = st == 0 ? 7 + SB : st == 1 ? 11 + 2 * SB : st == 2 ? 10 + SB : st == 3 ? 10 + SB
                         : st + 1 <= 15 ? 10 : st + 1 == 16 ? 5 : 0;
      wait_vm<NW>();
      stage_barrier();
      if constexpr (st == 0) poll_issue(pollB, hflg + 64 * 32);
      if constexpr (st == 2) flags_check(pollB, hflg + 64 * 32);
      if constexpr (PRE && st == 4) h_signal(hsigB, lane);
      if constexpr (st == 6) poll_issue(pollB, hflg + 128 * 32);
      if constexpr (st == 8) flags_check(pollB, hflg + 128 * 32);
      if constexpr (st == 12) poll_issue(pollY, ycnt);
      constexpr int dl = g_slot(st + 1) - g_slot(st);
#pragma unroll
      for (int t = 0; t < NT; ++t) wp[t] = opaque(wp[t] + dl);
      xp = opaque(xp + dl);
      lo = tr_half(wp[un], 0);
    }
    if constexpr (rd) { hi = tr_half(wp[un], 2 * 2304); wf[(U + 2) % 3] = join(lo, hi); }
    SCHED0();
    mm(2);
    if constexpr (u >= NU - 2 && st + 1 < 18) {
      xf[(st + 1) & 1][2 * (u - (NU - 2))] = ld_frag(xp, (2 * (u - (NU - 2))) * 2048);
      xf[(st + 1) & 1][2 * (u - (NU - 2)) + 1] = ld_frag(xp, (2 * (u - (NU - 2)) + 1) * 2048);
    }
    if constexpr (u == 0) { if constexpr (st + 3 < 18) g2_piece(g2, lane, st + 3, 2); }
    SCHED0();
    mm(3);
    if constexpr (u >= NU - 2) { if constexpr (st + 4 < 18) g2_piece(g2, lane, st + 4, u - (NU - 2)); }
    else if constexpr (u == 0) { if constexpr (st + 3 < 18) g2_piece(g2, lane, st + 3, 3); }
    else if constexpr (u == 1) { if constexpr (st + 3 < 18) g2_piece(g2, lane, st + 3, 4); }
    if constexpr (PRE && u == 1 && st < 2) {
#pragma unroll
      for (int mb = 2 * st; mb < 2 * st + 2; ++mb)
        __builtin_amdgcn_raw_buffer_store_b128(hB[mb], g2.sx, lane * 16, hBoff + mb * (144 * 1024), 16);
    }
    if constexpr (st == 17 && u >= 1) {
#pragma unroll
      for (int b2 = 0; b2 < 2; ++b2) *(v4f*)(xw + ((u - 1) * 2 + b2) * 1024) = kg ? acc[u - 1][b2] : acc[u - 1][2 + b2];
    }
    SCHED0();
  });
  mfma_drain();
  SCHED0();
#pragma unroll
  for (int b2 = 0; b2 < 2; ++b2) *(v4f*)(xw + ((NT - 1) * 2 + b2) * 1024) = kg ? acc[NT - 1][b2] : acc[NT - 1][2 + b2];
  stage_barrier();
  v4f r[2 * NT];
  {
    const char* rd = smem + ((wm * 2 + (kg ^ 1)) * 9 + tbase) * 2048 + lane * 16;
#pragma unroll
    for (int T = 0; T < NT; ++T)
#pragma unroll
      for (int b2 = 0; b2 < 2; ++b2) {
        const v4f o = *(const v4f*)(rd + (T * 2 + b2) * 1024);
        r[T * 2 + b2] = kg ? (o + acc[T][2 + b2]) : (acc[T][b2] + o);
      }
  }
  const srd_t sp = make_srd(ws + WS_PART, 64 * 4 * 8 * 4 * 3072);
#pragma unroll
  for (int f = 0; f < 2 * NT; ++f)
    if ((f & 3) != ksl)
      __builtin_amdgcn_raw_buffer_store_b128(__builtin_bit_cast(v4u, r[f]), sp, lane * 16,
                                             (u32)((((tile * 4 + (f & 3)) * 8 + w) * 4 + ksl) * 3072 + (f >> 2) * 1024), 16);
  v4f own[NO];
#pragma unroll
  for (int i = 0; i < NO; ++i) {
    const v4f a0 = r[4 * i], a1 = r[4 * i + 1];
    const v4f a2 = (4 * i + 2 < 2 * NT) ? r[(4 * i + 2 < 2 * NT) ? 4 * i + 2 : 0] : a0;
    const v4f a3 = (4 * i + 3 < 2 * NT) ? r[(4 * i + 3 < 2 * NT) ? 4 * i + 3 : 0] : a0;
    own[i] = ksl == 0 ? a0 : ksl == 1 ? a1 : ksl == 2 ? a2 : a3;
  }
  SCHED0();
  poll_check(pollY, ycnt, 576u);
  {
    const srd_t sy = make_srd(ws + WS_YT, S_ * D_ * 2);
    stt = ((const v2f*)(ws + WS_STATS))[tok0];
#pragma unroll
    for (int i = 0; i < NO; ++i) {
      const int dc = (4 * i + ksl < 2 * NT) ? dcol + 32 * i : dcol;
      xv[i] = *(const v2u*)(xin + (size_t)tok0 * D_ + dc);
#pragma unroll
      for (int r = 0; r < 4; ++r) yv[i][r] = __builtin_amdgcn_raw_buffer_load_b16(sy, (u32)(((dc + r) * S_ + tok0) * 2), 0, 16);
    }
  }
  SCHED0();
  wait_vm<NL>();
  u32* flags = (u32*)(ws + WS_CNT);
  if (lane < 4 && lane != ksl)
    __hip_atomic_store(flags + ((tile * 4 + lane) * 8 + w) * 16 + ksl, 1u, __ATOMIC_RELAXED, __HIP_MEMORY_SCOPE_AGENT);
  float xa[NO][4];
#pragma unroll
  for (int i = 0; i < NO; ++i)
#pragma unroll
    for (int e = 0; e < 4; ++e) {
      const float xf_ = (e & 1) ? bf_hi(xv[i][e >> 1]) : bf_lo(xv[i][e >> 1]);
      const float xn = rbf(rbf(xf_ - stt[0]) * stt[1]);
      xa[i][e] = rbf(xf_ + rbf(xn * bf_lo(yv[i][e])));
    }
  {
    u32* fl = flags + ((tile * 4 + ksl) * 8 + w) * 16;
    u32 spins = 0;
    for (;;) {
      const u32 v = (lane < 4 && lane != ksl) ? __hip_atomic_load(fl + lane, __ATOMIC_RELAXED, __HIP_MEMORY_SCOPE_AGENT) : 1u;
      if (__all(v != 0u)) break;
      __builtin_amdgcn_s_sleep(1);
      if (++spins > (1u << 24)) break;
    }
  }
  __builtin_amdgcn_fence(__ATOMIC_ACQUIRE, "wavefront");
  v4f pp[3][NO];
#pragma unroll
  for (int i3 = 0; i3 < 3; ++i3) {
    const int src = i3 + (i3 >= ksl ? 1 : 0);
    const u32 po = (u32)((((tile * 4 + ksl) * 8 + w) * 4 + src) * 3072);
#pragma unroll
    for (int i = 0; i < NO; ++i)
      pp[i3][i] = __builtin_bit_cast(v4f, __builtin_amdgcn_raw_buffer_load_b128(sp, lane * 16, po + i * 1024, 16));
  }
#pragma unroll
  for (int i = 0; i < NO; ++i) {
    const v4f s0 = ksl == 0 ? own[i] : pp[0][i];
    const v4f s1 = ksl == 1 ? own[i] : (ksl < 1 ? pp[0][i] : pp[1][i]);
    const v4f s2 = ksl == 2 ? own[i] : (ksl < 2 ? pp[1][i] : pp[2][i]);
    const v4f s3 = ksl == 3 ? own[i] : pp[2][i];
    const v4f B = (s0 + s1) + (s2 + s3);
    const float o0 = xa[i][0] + rbf(B[0]), o1 = xa[i][1] + rbf(B[1]);
    const float o2 = xa[i][2] + rbf(B[2]), o3 = xa[i][3] + rbf(B[3]);
    if (4 * i + ksl < 2 * NT)
      *(v2u*)(out + (size_t)tok0 * D_ + dcol + 32 * i) = v2u{pack_bf(o0, o1), pack_bf(o2, o3)};
  }
}

__global__ __launch_bounds__(512, 2) void fused_kernel(const u16* __restrict__ Wg, const u16* __restrict__ Wu,
                                                       const u16* __restrict__ Wd, const u16* __restrict__ ssmk,
                                                       const u16* __restrict__ ssmD, const u16* __restrict__ xin,
                                                       u16* __restrict__ out, char* __restrict__ ws) {
  extern __shared__ __attribute__((aligned(1024))) char smem[];
  const int bid = blockIdx.x;
  const int xcd = bid & 7, idx = bid >> 3;
  const int tid = threadIdx.x, lane = tid & 63;
  const int w = __builtin_amdgcn_readfirstlane(tid >> 6);
  const int ksl = xcd >> 1;
  const bool isconv = idx >= 24;
  const int tile2 = isconv ? (2 * (idx - 24) + (xcd & 1)) * 4 + 3 : (2 * (idx / 3) + (xcd & 1)) * 4 + idx % 3;
  const u32 sb = lds_addr(smem);
  G2Dma g2;
  g2.sw = make_srd(Wd, D_ * F_ * 2);
  g2.sx = make_srd(ws + WS_HT, S_ * F_ * 2);
  g2.wsoffA = (u32)((ksl * 1152 + 8 * (w & 3) + (w >> 2) * 64) * (D_ * 2) + (tile2 & 3) * 288);
  g2.wsoffB = (u32)((ksl * 1152 + 8 * w) * (D_ * 2) + (tile2 & 3) * 288);
  g2.xsoff = (u32)((((tile2 >> 2) * 8 + w) * 144 + ksl * 36) * 1024);
  g2.m0w = sb + w * 2304;
  g2.m0x = sb + G_XOFF + w * 2048;
  g2.woff = (u32)((lane >> 3) * (D_ * 2) + ((lane & 7) ^ (2 * (lane >> 4))) * 16);
  g2.soff = (u32)(((lane >> 1) & 7) * (D_ * 2) + (lane & 1) * 16);
  g2.xoff = lane * 16;
  const int kg2 = w >> 2, wm2 = (w >> 1) & 1, wn2 = (w & 1) ^ kg2;
  v4u hB[4];
  if (isconv) {
    const int cb = xcd * 8 + (idx - 24);
    conv_wg(smem, cb, w, lane, xcd, idx - 24, ssmk, ssmD, Wg, Wu, Wd, ws);
#pragma unroll
    for (int i = 0; i < 4; ++i) hB[i] = v4u{0u, 0u, 0u, 0u};
    if (wn2 == 0) gemm2_body<5, false>(smem, sb, g2, w, lane, kg2, wm2, 4, tile2, ksl, xin, out, ws, nullptr, hB, 0u);
    else          gemm2_body<4, false>(smem, sb, g2, w, lane, kg2, wm2, 0, tile2, ksl, xin, out, ws, nullptr, hB, 0u);
  } else {
    const int tile_n = xcd * 3 + idx % 3, tile_m = idx / 3;
    const int wm = w & 3, wn = w >> 2;
    const srd_t sw = make_srd((w >> 2) ? Wu : Wg, D_ * F_ * 2);
    const srd_t sx = make_srd(ws + WS_XNT, S_ * D_ * 2);
    const u32 wsoff = (u32)((8 * (w & 3)) * (F_ * 2) + tile_n * 384);
    const u32 xsoff = (u32)((tile_m * 16 + 2 * w) * (18 * 1024));
    u32* hsigA = (u32*)(ws + WS_HCNT) + ((2 * tile_m + (wm >> 1)) * 4 + ksl) * 32 + (tile_n % 6) * 4 + (wm & 1) * 2 + wn;
    const u32* hflg = (const u32*)(ws + WS_HCNT) + ((tile2 >> 2) * 4 + ksl) * 32 + (lane < 24 ? lane : 0);
    const u32 hoff = (u32)(((tile_m * 16 + wm * 4) * 144 + tile_n * 6) * 1024);
    gemm1_body(sb, sw, sx, wsoff, xsoff, w, lane, wm, wn, ws, hoff + 2 * wn * 1024, hsigA, g2, hflg, hB);
    const u32 hBoff = hoff + (4 + wn) * 1024;
    if (wn2 == 0) gemm2_body<5, true>(smem, sb, g2, w, lane, kg2, wm2, 4, tile2, ksl, xin, out, ws, hsigA + 128 * 32, hB, hBoff);
    else          gemm2_body<4, true>(smem, sb, g2, w, lane, kg2, wm2, 0, tile2, ksl, xin, out, ws, hsigA + 128 * 32, hB, hBoff);
  }
}

extern "C" void kernel_launch(void* const* d_in, const int* in_sizes, int n_in, void* d_out, int out_size,
                              void* d_ws, size_t ws_size, hipStream_t stream) {
  const u16* q = (const u16*)d_in[0];
  const u16* k = (const u16*)d_in[1];
  const u16* v = (const u16*)d_in[2];
  const u16* shk = (const u16*)d_in[3];
  const u16* ssmk = (const u16*)d_in[4];
  const u16* ssmD = (const u16*)d_in[5];
  const u16* Wg = (const u16*)d_in[7];
  const u16* Wu = (const u16*)d_in[8];
  const u16* Wd = (const u16*)d_in[9];
  char* ws = (char*)d_ws;
  (void)hipFuncSetAttribute((const void*)fused_kernel, hipFuncAttributeMaxDynamicSharedMemorySize, H_LDS);
  pre_kernel<<<256 + 288, 256, 0, stream>>>(q, k, v, shk, ws);
  fused_kernel<<<256, 512, H_LDS, stream>>>(Wg, Wu, Wd, ssmk, ssmD, q, (u16*)d_out, ws);
}
```

```cpp
#include <hip/hip_runtime.h>
#include <hip/hip_bf16.h>
#include <stdint.h>
#include <utility>

typedef unsigned short u16;
typedef unsigned int u32;
typedef short v4s __attribute__((ext_vector_type(4)));
typedef short v8s __attribute__((ext_vector_type(8)));
typedef float v4f __attribute__((ext_vector_type(4)));
typedef float v2f __attribute__((ext_vector_type(2)));
typedef u32 v4u __attribute__((ext_vector_type(4)));
typedef u32 v2u __attribute__((ext_vector_type(2)));
typedef __bf16 v2bf __attribute__((ext_vector_type(2)));
typedef __attribute__((address_space(3))) v4s* lds_v4s_ptr;

constexpr int S_ = 2048, D_ = 576, F_ = 4608;

#define DI __device__ __forceinline__

template <int... I, class Fn>
DI void static_for_impl(std::integer_sequence<int, I...>, Fn&& f) { (f(std::integral_constant<int, I>{}), ...); }
template <int N, class Fn>
DI void static_for(Fn&& f) { static_for_impl(std::make_integer_sequence<int, N>{}, f); }

DI float bf_lo(u32 w) { return __uint_as_float(w << 16); }
DI float bf_hi(u32 w) { return __uint_as_float(w & 0xffff0000u); }
DI float rbf(float f) { return (float)(__bf16)f; }
DI u32 pack_bf(float lo, float hi) {
  v2f t = {lo, hi};
  v2bf b = __builtin_convertvector(t, v2bf);
  return __builtin_bit_cast(u32, b);
}

template <int CTRL> DI float dpp_f(float v) {
  return __builtin_bit_cast(float, __builtin_amdgcn_update_dpp(0, __builtin_bit_cast(int, v), CTRL, 0xf, 0xf, false));
}
DI float sum32(float v) {
  v += dpp_f<0x128>(v);
  v += dpp_f<0x124>(v);
  v += dpp_f<0x122>(v);
  v += dpp_f<0x121>(v);
  return v + __shfl_xor(v, 16);
}
constexpr size_t WS_XNT = 0;
constexpr size_t WS_STATS = WS_XNT + (size_t)S_ * D_ * 2;
constexpr size_t WS_KVT = WS_STATS + (size_t)S_ * 8;
constexpr size_t WS_YT = WS_KVT + (size_t)S_ * D_ * 2;
constexpr size_t WS_HT = WS_YT + (size_t)S_ * D_ * 2;
constexpr size_t WS_PART = WS_HT + (size_t)S_ * F_ * 2;
constexpr size_t WS_CNT = WS_PART + (size_t)64 * 4 * 8 * 4 * 3072;
constexpr size_t WS_HCNT = WS_CNT + 2048 * 64;
constexpr size_t WS_YCNT = WS_HCNT + 3 * 64 * 128;

__global__ __launch_bounds__(256) void pre_kernel(const u16* __restrict__ x, const u16* __restrict__ kin,
                                                  const u16* __restrict__ vin, const u16* __restrict__ shk,
                                                  char* __restrict__ ws) {
  const int lane = threadIdx.x & 63, w = threadIdx.x >> 6;
  if (blockIdx.x < 8) *(v4u*)(ws + WS_CNT + (size_t)(blockIdx.x * 256 + threadIdx.x) * 64) = v4u{0u, 0u, 0u, 0u};
  else if (blockIdx.x < 15) *(v4u*)(ws + WS_HCNT + (size_t)((blockIdx.x - 8) * 256 + threadIdx.x) * 16) = v4u{0u, 0u, 0u, 0u};
  if (blockIdx.x < 256) {
    const int c = lane & 31;
    const int row = blockIdx.x * 8 + w * 2 + (lane >> 5);
    const v4u* src = (const v4u*)(x + (size_t)row * D_);
    v4u d[3];
    d[0] = src[c]; d[1] = src[c + 32]; d[2] = src[64 + (c & 7)];
    const float lastw = (c < 8) ? 1.f : 0.f;
    float f[3][8];
#pragma unroll
    for (int i = 0; i < 3; ++i)
#pragma unroll
      for (int j = 0; j < 4; ++j) { f[i][2 * j] = bf_lo(d[i][j]); f[i][2 * j + 1] = bf_hi(d[i][j]); }
    float s0 = 0.f, s1 = 0.f, s2 = 0.f;
#pragma unroll
    for (int j = 0; j < 8; ++j) { s0 += f[0][j]; s1 += f[1][j]; s2 += f[2][j]; }
    const float mean = sum32(s0 + s1 + s2 * lastw) * (1.0f / D_);
    float q0 = 0.f, q1 = 0.f, q2 = 0.f;
#pragma unroll
    for (int j = 0; j < 8; ++j) {
      const float a = f[0][j] - mean, b = f[1][j] - mean, e = f[2][j] - mean;
      q0 += a * a; q1 += b * b; q2 += e * e;
    }
    const float var = sum32(q0 + q1 + q2 * lastw) * (1.0f / D_);
    const float mb = rbf(mean);
    const float sd = rbf(sqrtf(rbf(rbf(var) + 1e-5f)));
    const float rinv = 1.0f / sd;
    if (c == 0) ((v2f*)(ws + WS_STATS))[row] = v2f{mb, rinv};
    char* xnt = ws + WS_XNT;
    const int tb = row >> 4, m = row & 15;
    const int cc = c & 3, T = cc >> 1, q0i = 2 * (cc & 1);
#pragma unroll
    for (int i = 0; i < 3; ++i) {
      u32 o[4];
#pragma unroll
      for (int j = 0; j < 4; ++j) {
        const u32 dd = pack_bf(f[i][2 * j] - mb, f[i][2 * j + 1] - mb);
        o[j] = pack_bf(bf_lo(dd) * rinv, bf_hi(dd) * rinv);
      }
      const int kb = 8 * i + (c >> 2);
      char* base = xnt + ((size_t)(tb * 18 + kb) * 64 + (q0i * 16 + m)) * 16 + 8 * T;
      if (i < 2 || c < 8) {
        *(v2u*)(base) = v2u{o[0], o[1]};
        *(v2u*)(base + 256) = v2u{o[2], o[3]};
      }
    }
  } else {
    const int b = blockIdx.x - 256;
    const int tt = b / 9, ct = b % 9;
    const int tg = lane >> 5, cx = lane & 31;
    const int s0 = tt * 64 + w * 16 + tg * 8, d0 = ct * 64 + cx * 2;
    u32 kk[11], vv[8];
#pragma unroll
    for (int i = 0; i < 11; ++i) {
      const int s = s0 - 3 + i;
      const u32 t = *(const u32*)(kin + (size_t)(s < 0 ? 0 : s) * D_ + d0);
      kk[i] = (i < 3 && s < 0) ? 0u : t;
    }
#pragma unroll
    for (int i = 0; i < 8; ++i) vv[i] = *(const u32*)(vin + (size_t)(s0 + i) * D_ + d0);
    const v4u w0 = *(const v4u*)(shk + (size_t)d0 * 4);
    u16* kvt = (u16*)(ws + WS_KVT);
#pragma unroll
    for (int ch = 0; ch < 2; ++ch) {
      const u32 wa = w0[2 * ch], wb = w0[2 * ch + 1];
      const float t0 = bf_lo(wa), t1 = bf_hi(wa), t2 = bf_lo(wb), t3 = bf_hi(wb);
      float kf[11];
#pragma unroll
      for (int i = 0; i < 11; ++i) kf[i] = ch ? bf_hi(kk[i]) : bf_lo(kk[i]);
      float r[8];
#pragma unroll
      for (int i = 0; i < 8; i += 2) {
        u32 a = pack_bf(kf[i] * t3, kf[i + 1] * t3);
        u32 p = pack_bf(kf[i + 1] * t2, kf[i + 2] * t2);
        a = pack_bf(bf_lo(a) + bf_lo(p), bf_hi(a) + bf_hi(p));
        p = pack_bf(kf[i + 2] * t1, kf[i + 3] * t1);
        a = pack_bf(bf_lo(a) + bf_lo(p), bf_hi(a) + bf_hi(p));
        p = pack_bf(kf[i + 3] * t0, kf[i + 4] * t0);
        a = pack_bf(bf_lo(a) + bf_lo(p), bf_hi(a) + bf_hi(p));
        r[i] = bf_lo(a) * (ch ? bf_hi(vv[i]) : bf_lo(vv[i]));
        r[i + 1] = bf_hi(a) * (ch ? bf_hi(vv[i + 1]) : bf_lo(vv[i + 1]));
      }
      v4u o = {pack_bf(r[7], r[6]), pack_bf(r[5], r[4]), pack_bf(r[3], r[2]), pack_bf(r[1], r[0])};
      *(v4u*)(kvt + (size_t)(d0 + ch) * S_ + s0) = o;
    }
  }
}

constexpr int G_STAGE = 34816, G_NBUF = 4, G_LDS = G_STAGE * G_NBUF;
constexpr int G_XOFF = 18432;

typedef __attribute__((address_space(3))) v8s* lds_v8s_ptr;
typedef __attribute__((address_space(3))) char* lds_ptr;
DI v8s tr_frag(u32 p, int off_lo, int off_hi) {
  v4s a = __builtin_amdgcn_ds_read_tr16_b64_v4i16((lds_v4s_ptr)((lds_ptr)p + off_lo));
  v4s b = __builtin_amdgcn_ds_read_tr16_b64_v4i16((lds_v4s_ptr)((lds_ptr)p + off_hi));
  return __builtin_shufflevector(a, b, 0, 1, 2, 3, 4, 5, 6, 7);
}
DI v4s tr_half(u32 p, int off) { return __builtin_amdgcn_ds_read_tr16_b64_v4i16((lds_v4s_ptr)((lds_ptr)p + off)); }
DI v8s join(const v4s& a, const v4s& b) { return __builtin_shufflevector(a, b, 0, 1, 2, 3, 4, 5, 6, 7); }
DI v8s ld_frag(u32 p, int off) { return *(lds_v8s_ptr)((lds_ptr)p + off); }
DI u32 opaque(u32 v) { asm volatile("" : "+v"(v)); return v; }
DI u32 lds_addr(const void* p) { return (u32)(size_t)(lds_ptr)p; }
typedef __amdgpu_buffer_rsrc_t srd_t;
DI srd_t make_srd(const void* p, u32 bytes) {
  return __builtin_amdgcn_make_buffer_rsrc((void*)p, 0, bytes, 0x00020000);
}
DI void dma16(u32 m0v, u32 voff, srd_t srd, u32 soff) {
  asm volatile("s_mov_b32 m0, %0\n\ts_nop 0\n\tbuffer_load_dwordx4 %1, %2, %3 offen lds"
               :: "s"(m0v), "v"(voff), "s"(srd), "s"(soff) : "memory");
}
DI void dma16_sc1(u32 m0v, u32 voff, srd_t srd, u32 soff) {
  asm volatile("s_mov_b32 m0, %0\n\ts_nop 0\n\tbuffer_load_dwordx4 %1, %2, %3 offen sc1 lds"
               :: "s"(m0v), "v"(voff), "s"(srd), "s"(soff) : "memory");
}
DI void wait_count(u32* p, u32 target) {
  u32 spins = 0;
  while (__hip_atomic_load(p, __ATOMIC_RELAXED, __HIP_MEMORY_SCOPE_AGENT) < target) {
    __builtin_amdgcn_s_sleep(1);
    if (++spins > (1u << 24)) break;
  }
  __builtin_amdgcn_fence(__ATOMIC_ACQUIRE, "wavefront");
}
#define SCHED0() __builtin_amdgcn_sched_barrier(0)
DI void mfma16(v4f& c, const v8s& a, const v8s& b) {
  asm volatile("v_mfma_f32_16x16x32_bf16 %0, %1, %2, %0" : "+v"(c) : "v"(a), "v"(b));
}
DI void mfma16_first(v4f& c, const v8s& a, const v8s& b) {
  asm volatile("v_mfma_f32_16x16x32_bf16 %0, %1, %2, 0" : "=&v"(c) : "v"(a), "v"(b));
}
DI void mfma_drain() { asm volatile("s_nop 15\n\ts_nop 15" ::: "memory"); }
template <int N> DI void wait_vm() { asm volatile("s_waitcnt vmcnt(%0)" :: "n"(N) : "memory"); }
DI void stage_barrier() { asm volatile("s_waitcnt lgkmcnt(0)\n\ts_barrier" ::: "memory"); }

constexpr int C_KBYTES = 2080 * 8, C_XBYTES = 8 * 768, C_WAVE = C_KBYTES + C_XBYTES;

DI void conv_channel(char* kl, int ch, int lane, const u16* __restrict__ ssmk, const u16* __restrict__ ssmD,
                     char* __restrict__ ws) {
  char* xl = kl + C_KBYTES;
  const srd_t sk = make_srd(ssmk + (size_t)ch * S_, S_ * 2);
  const v4u* xs = (const v4u*)(ws + WS_KVT + (size_t)ch * S_ * 2);
  v4u xd[4];
#pragma unroll
  for (int it = 0; it < 4; ++it) xd[it] = xs[it * 64 + lane];
  u32 d0[17], d1[17], d2[17];
#pragma unroll
  for (int it = 0; it < 17; ++it) {
    const int dw = it * 64 + lane - 16;
    if (it == 0 || it == 16) {
      d0[it] = __builtin_amdgcn_raw_buffer_load_b32(sk, (u32)(dw * 4), 0, 0);
      asm volatile("" ::: "memory");
      d1[it] = __builtin_amdgcn_raw_buffer_load_b32(sk, (u32)(dw * 4 + 4), 0, 0);
      asm volatile("" ::: "memory");
      d2[it] = __builtin_amdgcn_raw_buffer_load_b32(sk, (u32)(dw * 4 + 8), 0, 0);
    } else {
      typedef u32 v3u __attribute__((ext_vector_type(3)));
      const v3u t = __builtin_amdgcn_raw_buffer_load_b96(sk, (u32)(dw * 4), 0, 0);
      d0[it] = t[0]; d1[it] = t[1]; d2[it] = t[2];
    }
  }
#pragma unroll
  for (int it = 0; it < 2; ++it) {
    const int z = it * 64 + lane;
    *(v4u*)(xl + (z >> 4) * 768 + (z & 15) * 16) = v4u{0u, 0u, 0u, 0u};
  }
#pragma unroll
  for (int it = 0; it < 4; ++it) {
    const int cidx = it * 64 + lane;
    *(v4u*)(xl + (cidx & 7) * 768 + ((cidx >> 3) + 16) * 16) = xd[it];
  }
#pragma unroll
  for (int it = 0; it < 17; ++it) {
    const int L = it * 64 + lane;
    v4u o = {d0[it], d1[it], __builtin_amdgcn_alignbit(d1[it], d0[it], 16), __builtin_amdgcn_alignbit(d2[it], d1[it], 16)};
    if (it < 16 || lane < 16) *(v4u*)(kl + L * 16) = o;
  }
  __builtin_amdgcn_s_waitcnt(0xC07F);
  __builtin_amdgcn_wave_barrier();

  const int i16 = lane & 15, q = lane >> 4;
  const u32 ap = opaque(lds_addr(kl) + (i16 - 8 * q - 7 + 32) * 8);
  const u32 bp = opaque(lds_addr(xl) + q * 768 + i16 * 16);
  v4f acc[4][2];
  v8s af[3][4], bfr[2][4];
  auto rdA = [&](int c, int j) -> v8s {
    const int delta = 64 * c + 16 * j;
    v4s lo = *(volatile __attribute__((address_space(3))) v4s*)((lds_ptr)ap + 8 * delta);
    v4s hi = *(volatile __attribute__((address_space(3))) v4s*)((lds_ptr)ap + 8 * delta + 32);
    return __builtin_shufflevector(lo, hi, 0, 1, 2, 3, 4, 5, 6, 7);
  };
  auto rdB = [&](int c, int n, int kb) -> v8s { return ld_frag(bp, 4 * kb * 768 + 16 * (16 * n - c + 16)); };
#pragma unroll
  for (int j = 0; j < 4; ++j) af[0][j] = rdA(0, j);
#pragma unroll
  for (int j = 0; j < 4; ++j) bfr[0][j] = rdB(0, j >> 1, j & 1);
  SCHED0();
  static_for<32>([&](auto CC) {
    constexpr int c = decltype(CC)::value;
    if constexpr (c + 1 < 32) {
#pragma unroll
      for (int j = 0; j < 4; ++j) af[(c + 1) % 3][j] = rdA(c + 1, j);
#pragma unroll
      for (int j = 0; j < 4; ++j)
        if (j >= 2 || c + 1 < 16) bfr[(c + 1) & 1][j] = rdB(c + 1, j >> 1, j & 1);
    }
    SCHED0();
#pragma unroll
    for (int n = 1; n >= 0; --n) {
      if (n == 0 && c >= 16) continue;
#pragma unroll
      for (int kb = 0; kb < 2; ++kb) {
#pragma unroll
        for (int ib = 0; ib < 4; ++ib) {
          const int j = ib - 2 * kb;
          if (c == 0 && j < 0) continue;
          const v8s& A = (j >= 0) ? af[c % 3][j >= 0 ? j : 0] : af[(c + 2) % 3][j < 0 ? j + 4 : 0];
          if (c == 0 && kb == 0) mfma16_first(acc[ib][n], A, bfr[c & 1][2 * n + kb]);
          else mfma16(acc[ib][n], A, bfr[c & 1][2 * n + kb]);
        }
      }
    }
    SCHED0();
  });
  mfma_drain();
  SCHED0();
  const float Dd = bf_lo((u32)ssmD[ch]);
  const srd_t sy = make_srd(ws + WS_YT + (size_t)ch * S_ * 2, S_ * 2);
#pragma unroll
  for (int n = 0; n < 2; ++n)
#pragma unroll
    for (int ib = 0; ib < 4; ++ib) {
      const int beta = 16 * n + i16;
      const v2u kvp = *(const v2u*)(xl + (2 * ib + (q >> 1)) * 768 + (beta + 16) * 16 + 8 * (1 - (q & 1)));
      const float k3 = bf_lo(kvp[0]), k2 = bf_hi(kvp[0]), k1 = bf_lo(kvp[1]), k0 = bf_hi(kvp[1]);
      const float y0 = rbf(acc[ib][n][0]) + rbf(k0 * Dd);
      const float y1 = rbf(acc[ib][n][1]) + rbf(k1 * Dd);
      const float y2 = rbf(acc[ib][n][2]) + rbf(k2 * Dd);
      const float y3 = rbf(acc[ib][n][3]) + rbf(k3 * Dd);
      __builtin_amdgcn_raw_buffer_store_b64(v2u{pack_bf(y0, y1), pack_bf(y2, y3)}, sy, (u32)((64 * beta + 16 * ib + 4 * q) * 2), 0, 16);
    }
  asm volatile("s_waitcnt vmcnt(0) lgkmcnt(0)" ::: "memory");
  if (lane == 0) __hip_atomic_fetch_add((u32*)(ws + WS_YCNT), 1u, __ATOMIC_RELAXED, __HIP_MEMORY_SCOPE_AGENT);
  __builtin_amdgcn_wave_barrier();
}

constexpr int H_LDS = 163840;
constexpr int A_STAGE = 32768;
constexpr int b_slot(int s) { return (s % 3) == 0 ? 65536 : (s % 3) == 1 ? 114688 : 0; }
constexpr int g_slot(int s) { return (s & 3) == 0 ? 49152 : (s & 3) == 1 ? 83968 : (s & 3) == 2 ? 118784 : 0; }
constexpr int gb_store(int st) { return st == 1 ? 2 : st == 2 ? 2 : (st >= 3 && st <= 6) ? 1 : 0; }
constexpr int gb_extra(int st) { return (st == 4 || st == 6) ? 1 : 0; }
constexpr int gb_wait(int st) {
  return st >= 7 ? 0 : (st > 0 ? gb_store(st - 1) + gb_extra(st - 1) : 0) + 6 + gb_store(st);
}

DI void h_signal(u32* p, int lane) {
  if (lane == 0) __hip_atomic_store(p, 1u, __ATOMIC_RELAXED, __HIP_MEMORY_SCOPE_AGENT);
}
DI void wait_flags(const u32* p) {
  u32 spins = 0;
  while (!__all(__hip_atomic_load(p, __ATOMIC_RELAXED, __HIP_MEMORY_SCOPE_AGENT) != 0u)) {
    __builtin_amdgcn_s_sleep(1);
    if (++spins > (1u << 24)) break;
  }
  __builtin_amdgcn_fence(__ATOMIC_ACQUIRE, "wavefront");
}
DI u32 h_dword(float g0, float g1, float u0, float u1) {
  const u32 g = pack_bf(g0, g1), u = pack_bf(u0, u1);
  return pack_bf(bf_lo(g) * bf_lo(u), bf_hi(g) * bf_hi(u));
}
DI void poll_issue(u32& v, const u32* p) {
  asm volatile("global_load_dword %0, %1, off sc1" : "=v"(v) : "v"(p) : "memory");
}
DI void poll_check(u32& v, u32* p, u32 target) {
  asm volatile("" : "+v"(v));
  if (__builtin_amdgcn_readfirstlane(v) < target) wait_count(p, target);
}

DI void flags_check(u32& v, const u32* p) {
  asm volatile("" : "+v"(v));
  if (!__all(v != 0u)) wait_flags(p);
}

struct G2Dma {
  srd_t sw, sx;
  u32 wsoffA, wsoffB, xsoff, m0w, m0x, woff, soff, xoff;
};
DI void g2_piece(const G2Dma& d, int lane, int st, int i) {
  const u32 bo = g_slot(st);
  const int kb = st < 6 ? 6 * st : st < 12 ? 6 * (st - 6) + 1 : 6 * (st - 12) + 4;
  const int kb2 = kb + (st < 12 ? 2 : 1);
  const u32 wo = (st < 12 ? d.wsoffA : d.wsoffB) + (u32)kb * (32 * D_ * 2);
  if (i == 0) dma16(d.m0w + bo, d.woff, d.sw, wo);
  else if (i == 1) dma16(d.m0w + bo + 1024, d.woff, d.sw, wo + 128);
  else if (i == 2) { if (lane < 16) dma16(d.m0w + bo + 2048, d.soff, d.sw, wo + 256); }
  else if (i == 3) dma16_sc1(d.m0x + bo, d.xoff, d.sx, d.xsoff + kb * 1024);
  else dma16_sc1(d.m0x + bo + 1024, d.xoff, d.sx, d.xsoff + kb2 * 1024);
}

DI void gemm1_body(u32 sb, srd_t sw, srd_t sx, u32 wsoff, u32 xsoff, int w, int lane, int wm, int wn,
                   char* ws, u32 hoffA, u32* hsig, const G2Dma& g2, const u32* hflg, v4u (&hB)[4]) {
  constexpr int NU = 8;
  const int g = lane >> 4, qq = (lane & 15) >> 2, p = lane & 3;
  const int rr = 4 * (g & 1) + qq;
  const int wl0 = rr * 128 + (2 * (rr >> 1)) * 16 + p * 8;
  const u32 woff = (u32)((lane >> 3) * (F_ * 2) + ((lane & 7) ^ (2 * (lane >> 4))) * 16);
  const u32 xoff = lane * 16;
  v8s wf[3], xf[2][4];
  const u32 m0wB = sb + (w & 3) * 2048 + (w >> 2) * 1024;
  const u32 m0xB = sb + 16384 + w * 4096;
  auto dmaB = [&](int st, int i) {
    const u32 bo = b_slot(st);
    if (i < 2) dma16(m0wB + bo + i * 8192, woff, sw, wsoff + (u32)st * (64 * F_ * 2) + 256 + i * (32 * F_ * 2));
    else dma16(m0xB + bo + (i - 2) * 1024, xoff, sx, xsoff + st * 2048 + ((i - 2) >> 1) * (18 * 1024) + ((i - 2) & 1) * 1024);
  };
  v4f accA[2][4][4];
  {
    u32 wp[4], xp;
#pragma unroll
    for (int t = 0; t < 4; ++t) wp[t] = opaque((sb + wl0 + (g >> 1) * 4096 + wn * 1024) ^ (t << 5));
    xp = opaque(sb + 16384 + wm * 4096 + lane * 16);
    const u32 m0w = sb + (w & 3) * 4096 + (w >> 2) * 2048;
    const u32 m0x = sb + 16384 + w * 2048;
    auto dma_piece = [&](int st, int i) {
      const u32 bo = (st & 3) * A_STAGE;
      if (i < 2) dma16(m0w + bo + i * 1024, woff, sw, wsoff + (u32)st * (32 * F_ * 2) + i * 128);
      else dma16(m0x + bo + (i - 2) * 1024, xoff, sx, xsoff + st * 1024 + (i - 2) * (18 * 1024));
    };
#pragma unroll
    for (int st = 0; st < 3; ++st)
#pragma unroll
      for (int i = 0; i < 4; ++i) dma_piece(st, i);
    wait_vm<8>();
    stage_barrier();
#pragma unroll
    for (int mb = 0; mb < 4; ++mb) xf[0][mb] = ld_frag(xp, mb * 1024);
    wf[0] = tr_frag(wp[0], 0, 8192);
    wf[1] = tr_frag(wp[0], 2048, 2048 + 8192);
    dma_piece(3, 0); dma_piece(3, 1);
    SCHED0();
    static_for<18 * NU>([&](auto UC) {
      constexpr int U = decltype(UC)::value, st = U / NU, u = U % NU;
      constexpr int T = u >> 1, gu = u & 1;
      constexpr bool trans = (u == NU - 2 && st + 1 < 18);
      constexpr bool rd = (u + 2 < NU) || (st + 1 < 18);
      constexpr int un = (u + 2) % NU;
      auto mm = [&](int mb) {
        if constexpr (st == 0) mfma16_first(accA[gu][T][mb], wf[U % 3], xf[st & 1][mb]);
        else mfma16(accA[gu][T][mb], wf[U % 3], xf[st & 1][mb]);
        SCHED0();
      };
      v4s lo, hi;
      mm(0);
      if constexpr (!trans && rd) { lo = tr_half(wp[un >> 1], (un & 1) * 2048); SCHED0(); }
      mm(1);
      if constexpr (trans) {
        if constexpr (st + 1 <= 15) wait_vm<8>(); else if constexpr (st + 1 == 16) wait_vm<4>(); else wait_vm<8>();
        stage_barrier();
        constexpr int dl = (((st + 1) & 3) == 0) ? -3 * A_STAGE : A_STAGE;
#pragma unroll
        for (int t = 0; t < 4; ++t) wp[t] = opaque(wp[t] + dl);
        xp = opaque(xp + dl);
        lo = tr_half(wp[un >> 1], (un & 1) * 2048);
      }
      if constexpr (rd) { hi = tr_half(wp[un >> 1], (un & 1) * 2048 + 8192); wf[(U + 2) % 3] = join(lo, hi); }
      SCHED0();
      mm(2);
      if constexpr (u >= NU - 2 && st + 1 < 18) {
        xf[(st + 1) & 1][2 * (u - 6)] = ld_frag(xp, (2 * (u - 6)) * 1024);
        xf[(st + 1) & 1][2 * (u - 6) + 1] = ld_frag(xp, (2 * (u - 6) + 1) * 1024);
      }
      SCHED0();
      mm(3);
      if constexpr (u >= NU - 2) { if constexpr (st + 4 < 18) dma_piece(st + 4, u - (NU - 2)); }
      else if constexpr (u < 2) { if constexpr (st + 3 < 18) dma_piece(st + 3, u + 2); }
      constexpr int qB = U - (15 * NU + 6);
      if constexpr (qB >= 0 && qB < 12) dmaB(qB / 6, qB % 6);
      SCHED0();
    });
  }
  {
    v4f acc[2][2][4];
    v4u hA[8];
    u32 wp[2], xp, pollA = 0;
#pragma unroll
    for (int t = 0; t < 2; ++t) wp[t] = opaque((sb + b_slot(0) + wl0 + (g >> 1) * 2048) ^ ((2 * wn + t) << 5));
    xp = opaque(sb + b_slot(0) + 16384 + wm * 8192 + lane * 16);
    auto storeA = [&](int i) {
      __builtin_amdgcn_raw_buffer_store_b128(hA[i], g2.sx, lane * 16, hoffA + (i & 3) * (144 * 1024) + (i >> 2) * 1024, 16);
    };
    auto conv1 = [&](int cq) {
      const int gi = cq >> 2, pr = gi >> 2, mb = gi & 3, cT = 2 * pr + ((cq >> 1) & 1), ch = cq & 1;
      hA[gi][cq & 3] = h_dword(accA[0][cT][mb][2 * ch], accA[0][cT][mb][2 * ch + 1], accA[1][cT][mb][2 * ch], accA[1][cT][mb][2 * ch + 1]);
    };
    wait_vm<6>();
    stage_barrier();
#pragma unroll
    for (int mb = 0; mb < 4; ++mb) xf[0][mb] = ld_frag(xp, mb * 2048);
    wf[0] = tr_frag(wp[0], 0, 4096);
    wf[1] = tr_frag(wp[0], 1024, 1024 + 4096);
    dmaB(2, 0); dmaB(2, 1);
    mfma_drain();
    SCHED0();
    static_for<9 * NU>([&](auto UC) {
      constexpr int U = decltype(UC)::value, st = U / NU, u = U % NU;
      constexpr int T = (u >> 1) & 1, gu = u & 1;
      constexpr bool trans = (u == NU - 2 && st + 1 < 9);
      constexpr bool rd = (u + 2 < NU) || (st + 1 < 9);
      constexpr int un = (u + 2) % NU;
      constexpr int wo = (un >> 2) * 8192 + (un & 1) * 1024;
      auto mm = [&](int mb) {
        if constexpr (st == 0 && u < 4) mfma16_first(acc[gu][T][mb], wf[U % 3], xf[u >> 2][mb]);
        else mfma16(acc[gu][T][mb], wf[U % 3], xf[u >> 2][mb]);
        SCHED0();
      };
      v4s lo, hi;
      mm(0);
      if constexpr (!trans && rd) { lo = tr_half(wp[(un >> 1) & 1], wo); }
      if constexpr (U < 16) conv1(2 * U);
      SCHED0();
      mm(1);
      if constexpr (trans) {
        wait_vm<gb_wait(st)>();
        stage_barrier();
        if constexpr (st == 4) h_signal(hsig, lane);
        if constexpr (st == 6) poll_issue(pollA, hflg);
        if constexpr (st == 7) { h_signal(hsig + 64 * 32, lane); flags_check(pollA, hflg); }
        constexpr int dl = b_slot(st + 1) - b_slot(st);
#pragma unroll
        for (int t = 0; t < 2; ++t) wp[t] = opaque(wp[t] + dl);
        xp = opaque(xp + dl);
        lo = tr_half(wp[(un >> 1) & 1], wo);
      }
      if constexpr (rd) { hi = tr_half(wp[(un >> 1) & 1], wo + 2 * 2048); wf[(U + 2) % 3] = join(lo, hi); }
      SCHED0();
      mm(2);
      if constexpr (u == 1 || u == 2) { xf[1][2 * (u - 1)] = ld_frag(xp, (2 * (u - 1)) * 2048 + 1024); xf[1][2 * (u - 1) + 1] = ld_frag(xp, (2 * (u - 1) + 1) * 2048 + 1024); }
      if constexpr (u >= NU - 2 && st + 1 < 9) { xf[0][2 * (u - 6)] = ld_frag(xp, (2 * (u - 6)) * 2048); xf[0][2 * (u - 6) + 1] = ld_frag(xp, (2 * (u - 6) + 1) * 2048); }
      if constexpr (U < 16) conv1(2 * U + 1);
      SCHED0();
      mm(3);
      if constexpr (u >= NU - 2) { if constexpr (st + 3 < 9) dmaB(st + 3, u - (NU - 2)); }
      else if constexpr (u < 4) { if constexpr (st + 2 < 9) dmaB(st + 2, u + 2); }
      if constexpr ((st == 1 || st == 2) && (u == 4 || u == 5)) storeA(2 * (st - 1) + u - 4);
      if constexpr (st >= 3 && st <= 6 && u == 4) storeA(st + 1);
      constexpr int qG = U - (7 * NU + 6);
      if constexpr (qG >= 0 && qG < 4) { g2_piece(g2, lane, (2 * qG) / 3, (2 * qG) % 3); g2_piece(g2, lane, (2 * qG + 1) / 3, (2 * qG + 1) % 3); }
      if constexpr (qG == 4) g2_piece(g2, lane, 2, 2);
      if constexpr (qG >= 5 && qG < 8) { g2_piece(g2, lane, qG - 5, 3); g2_piece(g2, lane, qG - 5, 4); }
      SCHED0();
    });
    mfma_drain();
    SCHED0();
#pragma unroll
    for (int mb = 0; mb < 4; ++mb)
#pragma unroll
      for (int c = 0; c < 4; ++c)
        hB[mb][c] = h_dword(acc[0][c >> 1][mb][2 * (c & 1)], acc[0][c >> 1][mb][2 * (c & 1) + 1],
                            acc[1][c >> 1][mb][2 * (c & 1)], acc[1][c >> 1][mb][2 * (c & 1) + 1]);
  }
}

template <int NT, bool PRE>
DI void gemm2_body(char* smem, u32 sb, const G2Dma& g2, int w, int lane, int kg, int wm, int tbase, int tile, int ksl,
                   const u16* __restrict__ xin, u16* __restrict__ out, char* ws, u32* hsigB, const v4u (&hB)[4],
                   u32 hBoff) {
  constexpr int NU = NT;
  constexpr int NO = (2 * NT + 3) / 4;
  v4f acc[NT][4];
  const int g = lane >> 4, qq = (lane & 15) >> 2, p = lane & 3;
  const int rr = 4 * (g & 1) + qq;
  const int wl0 = rr * 128 + (2 * (rr >> 1)) * 16 + p * 8;
  u32 wp[NT], xp;
#pragma unroll
  for (int t = 0; t < 4; ++t)
    wp[t] = opaque((sb + g_slot(0) + wl0 + ((g >> 1) + 4 * kg) * 2304 + (NT == 5 ? 1024 : 0)) ^ (t << 5));
  if constexpr (NT == 5) wp[4] = opaque(sb + g_slot(0) + 2048 + rr * 32 + p * 8 + ((g >> 1) + 4 * kg) * 2304);
  xp = opaque(sb + g_slot(0) + G_XOFF + wm * 8192 + kg * 1024 + lane * 16);
  auto rdW = [&](int u) -> v8s { return tr_frag(wp[u], 0, 2 * 2304); };
  const u32* hflg = (const u32*)(ws + WS_HCNT) + ((tile >> 2) * 4 + ksl) * 32 + (lane < 24 ? lane : 0);
  u32* ycnt = (u32*)(ws + WS_YCNT);
  v8s wf[3], xf[2][4];
  u32 pollB = 0, pollY = 0;
  const int j = 2 * wm + kg;
  const int tok0 = (tile >> 2) * 128 + j * 32 + (lane & 15) + 16 * (ksl & 1);
  const int dcol = (tile & 3) * 144 + tbase * 16 + (lane >> 4) * 4 + 16 * (ksl >> 1);
  v2u xv[NO]; u32 yv[NO][4]; v2f stt;
  constexpr int NL = 1 + 5 * NO;
  char* xw = smem + ((wm * 2 + kg) * 9 + tbase) * 2048 + lane * 16;
  if constexpr (!PRE) {
#pragma unroll
    for (int st = 0; st < 3; ++st)
#pragma unroll
      for (int i = 0; i < 3; ++i) g2_piece(g2, lane, st, i);
    wait_flags(hflg);
#pragma unroll
    for (int st = 0; st < 3; ++st) { g2_piece(g2, lane, st, 3); g2_piece(g2, lane, st, 4); }
  }
  wait_vm<4>();
  stage_barrier();
#pragma unroll
  for (int mb = 0; mb < 4; ++mb) xf[0][mb] = ld_frag(xp, mb * 2048);
  wf[0] = rdW(0);
  wf[1] = rdW(1);
  g2_piece(g2, lane, 3, 0); g2_piece(g2, lane, 3, 1);
  SCHED0();
  static_for<18 * NU>([&](auto UC) {
    constexpr int U = decltype(UC)::value, st = U / NU, u = U % NU;
    constexpr int SB = PRE ? 2 : 0;
    constexpr bool trans = (u == NU - 2 && st + 1 < 18);
    constexpr bool rd = (u + 2 < NU) || (st + 1 < 18);
    constexpr int un = (u + 2) % NU;
    auto mm = [&](int mb) {
      if constexpr (st == 0) mfma16_first(acc[u][mb], wf[U % 3], xf[st & 1][mb]);
      else mfma16(acc[u][mb], wf[U % 3], xf[st & 1][mb]);
      SCHED0();
    };
    v4s lo, hi;
    mm(0);
    if constexpr (!trans && rd) { lo = tr_half(wp[un], 0); SCHED0(); }
    mm(1);
    if constexpr (trans) {
      constexpr int NW = st == 0 ? 7 + SB : st == 1 ? 11 + 2 * SB : st == 2 ? 10 + SB : st == 3 ? 10 + SB
                         : st + 1 <= 15 ? 10 : st + 1 == 16 ? 5 : 0;
      wait_vm<NW>();
      stage_barrier();
      if constexpr (st == 0) poll_issue(pollB, hflg + 64 * 32);
      if constexpr (st == 2) flags_check(pollB, hflg + 64 * 32);
      if constexpr (PRE && st == 4) h_signal(hsigB, lane);
      if constexpr (st == 6) poll_issue(pollB, hflg + 128 * 32);
      if constexpr (st == 8) flags_check(pollB, hflg + 128 * 32);
      if constexpr (st == 12) poll_issue(pollY, ycnt);
      constexpr int dl = g_slot(st + 1) - g_slot(st);
#pragma unroll
      for (int t = 0; t < NT; ++t) wp[t] = opaque(wp[t] + dl);
      xp = opaque(xp + dl);
      lo = tr_half(wp[un], 0);
    }
    if constexpr (rd) { hi = tr_half(wp[un], 2 * 2304); wf[(U + 2) % 3] = join(lo, hi); }
    SCHED0();
    mm(2);
    if constexpr (u >= NU - 2 && st + 1 < 18) {
      xf[(st + 1) & 1][2 * (u - (NU - 2))] = ld_frag(xp, (2 * (u - (NU - 2))) * 2048);
      xf[(st + 1) & 1][2 * (u - (NU - 2)) + 1] = ld_frag(xp, (2 * (u - (NU - 2)) + 1) * 2048);
    }
    if constexpr (u == 0) { if constexpr (st + 3 < 18) g2_piece(g2, lane, st + 3, 2); }
    SCHED0();
    mm(3);
    if constexpr (u >= NU - 2) { if constexpr (st + 4 < 18) g2_piece(g2, lane, st + 4, u - (NU - 2)); }
    else if constexpr (u == 0) { if constexpr (st + 3 < 18) g2_piece(g2, lane, st + 3, 3); }
    else if constexpr (u == 1) { if constexpr (st + 3 < 18) g2_piece(g2, lane, st + 3, 4); }
    if constexpr (PRE && u == 1 && st < 2) {
#pragma unroll
      for (int mb = 2 * st; mb < 2 * st + 2; ++mb)
        __builtin_amdgcn_raw_buffer_store_b128(hB[mb], g2.sx, lane * 16, hBoff + mb * (144 * 1024), 16);
    }
    if constexpr (st == 17 && u >= 1) {
#pragma unroll
      for (int b2 = 0; b2 < 2; ++b2) *(v4f*)(xw + ((u - 1) * 2 + b2) * 1024) = kg ? acc[u - 1][b2] : acc[u - 1][2 + b2];
    }
    SCHED0();
  });
  mfma_drain();
  SCHED0();
#pragma unroll
  for (int b2 = 0; b2 < 2; ++b2) *(v4f*)(xw + ((NT - 1) * 2 + b2) * 1024) = kg ? acc[NT - 1][b2] : acc[NT - 1][2 + b2];
  stage_barrier();
  v4f r[2 * NT];
  {
    const char* rd = smem + ((wm * 2 + (kg ^ 1)) * 9 + tbase) * 2048 + lane * 16;
#pragma unroll
    for (int T = 0; T < NT; ++T)
#pragma unroll
      for (int b2 = 0; b2 < 2; ++b2) {
        const v4f o = *(const v4f*)(rd + (T * 2 + b2) * 1024);
        r[T * 2 + b2] = kg ? (o + acc[T][2 + b2]) : (acc[T][b2] + o);
      }
  }
  const srd_t sp = make_srd(ws + WS_PART, 64 * 4 * 8 * 4 * 3072);
#pragma unroll
  for (int f = 0; f < 2 * NT; ++f)
    if ((f & 3) != ksl)
      __builtin_amdgcn_raw_buffer_store_b128(__builtin_bit_cast(v4u, r[f]), sp, lane * 16,
                                             (u32)((((tile * 4 + (f & 3)) * 8 + w) * 4 + ksl) * 3072 + (f >> 2) * 1024), 16);
  v4f own[NO];
#pragma unroll
  for (int i = 0; i < NO; ++i) {
    const v4f a0 = r[4 * i], a1 = r[4 * i + 1];
    const v4f a2 = (4 * i + 2 < 2 * NT) ? r[(4 * i + 2 < 2 * NT) ? 4 * i + 2 : 0] : a0;
    const v4f a3 = (4 * i + 3 < 2 * NT) ? r[(4 * i + 3 < 2 * NT) ? 4 * i + 3 : 0] : a0;
    own[i] = ksl == 0 ? a0 : ksl == 1 ? a1 : ksl == 2 ? a2 : a3;
  }
  SCHED0();
  poll_check(pollY, ycnt, 576u);
  {
    const srd_t sy = make_srd(ws + WS_YT, S_ * D_ * 2);
    stt = ((const v2f*)(ws + WS_STATS))[tok0];
#pragma unroll
    for (int i = 0; i < NO; ++i) {
      const int dc = (4 * i + ksl < 2 * NT) ? dcol + 32 * i : dcol;
      xv[i] = *(const v2u*)(xin + (size_t)tok0 * D_ + dc);
#pragma unroll
      for (int r = 0; r < 4; ++r) yv[i][r] = __builtin_amdgcn_raw_buffer_load_b16(sy, (u32)(((dc + r) * S_ + tok0) * 2), 0, 16);
    }
  }
  SCHED0();
  wait_vm<NL>();
  u32* flags = (u32*)(ws + WS_CNT);
  if (lane < 4 && lane != ksl)
    __hip_atomic_store(flags + ((tile * 4 + lane) * 8 + w) * 16 + ksl, 1u, __ATOMIC_RELAXED, __HIP_MEMORY_SCOPE_AGENT);
  float xa[NO][4];
#pragma unroll
  for (int i = 0; i < NO; ++i)
#pragma unroll
    for (int e = 0; e < 4; ++e) {
      const float xf_ = (e & 1) ? bf_hi(xv[i][e >> 1]) : bf_lo(xv[i][e >> 1]);
      const float xn = rbf(rbf(xf_ - stt[0]) * stt[1]);
      xa[i][e] = rbf(xf_ + rbf(xn * bf_lo(yv[i][e])));
    }
  {
    u32* fl = flags + ((tile * 4 + ksl) * 8 + w) * 16;
    u32 spins = 0;
    for (;;) {
      const u32 v = (lane < 4 && lane != ksl) ? __hip_atomic_load(fl + lane, __ATOMIC_RELAXED, __HIP_MEMORY_SCOPE_AGENT) : 1u;
      if (__all(v != 0u)) break;
      __builtin_amdgcn_s_sleep(1);
      if (++spins > (1u << 24)) break;
    }
  }
  __builtin_amdgcn_fence(__ATOMIC_ACQUIRE, "wavefront");
  v4f pp[3][NO];
#pragma unroll
  for (int i3 = 0; i3 < 3; ++i3) {
    const int src = i3 + (i3 >= ksl ? 1 : 0);
    const u32 po = (u32)((((tile * 4 + ksl) * 8 + w) * 4 + src) * 3072);
#pragma unroll
    for (int i = 0; i < NO; ++i)
      pp[i3][i] = __builtin_bit_cast(v4f, __builtin_amdgcn_raw_buffer_load_b128(sp, lane * 16, po + i * 1024, 16));
  }
#pragma unroll
  for (int i = 0; i < NO; ++i) {
    const v4f s0 = ksl == 0 ? own[i] : pp[0][i];
    const v4f s1 = ksl == 1 ? own[i] : (ksl < 1 ? pp[0][i] : pp[1][i]);
    const v4f s2 = ksl == 2 ? own[i] : (ksl < 2 ? pp[1][i] : pp[2][i]);
    const v4f s3 = ksl == 3 ? own[i] : pp[2][i];
    const v4f B = (s0 + s1) + (s2 + s3);
    const float o0 = xa[i][0] + rbf(B[0]), o1 = xa[i][1] + rbf(B[1]);
    const float o2 = xa[i][2] + rbf(B[2]), o3 = xa[i][3] + rbf(B[3]);
    if (4 * i + ksl < 2 * NT)
      *(v2u*)(out + (size_t)tok0 * D_ + dcol + 32 * i) = v2u{pack_bf(o0, o1), pack_bf(o2, o3)};
  }
}

__global__ __launch_bounds__(512, 2) void fused_kernel(const u16* __restrict__ Wg, const u16* __restrict__ Wu,
                                                       const u16* __restrict__ Wd, const u16* __restrict__ ssmk,
                                                       const u16* __restrict__ ssmD, const u16* __restrict__ xin,
                                                       u16* __restrict__ out, char* __restrict__ ws) {
  extern __shared__ __attribute__((aligned(1024))) char smem[];
  const int bid = blockIdx.x;
  const int xcd = bid & 7, idx = bid >> 3;
  const int tid = threadIdx.x, lane = tid & 63;
  const int w = __builtin_amdgcn_readfirstlane(tid >> 6);
  const int ksl = xcd >> 1;
  const bool isconv = idx >= 24;
  const int tile2 = isconv ? (2 * (idx - 24) + (xcd & 1)) * 4 + 3 : (2 * (idx / 3) + (xcd & 1)) * 4 + idx % 3;
  const u32 sb = lds_addr(smem);
  G2Dma g2;
  g2.sw = make_srd(Wd, D_ * F_ * 2);
  g2.sx = make_srd(ws + WS_HT, S_ * F_ * 2);
  g2.wsoffA = (u32)((ksl * 1152 + 8 * (w & 3) + (w >> 2) * 64) * (D_ * 2) + (tile2 & 3) * 288);
  g2.wsoffB = (u32)((ksl * 1152 + 8 * w) * (D_ * 2) + (tile2 & 3) * 288);
  g2.xsoff = (u32)((((tile2 >> 2) * 8 + w) * 144 + ksl * 36) * 1024);
  g2.m0w = sb + w * 2304;
  g2.m0x = sb + G_XOFF + w * 2048;
  g2.woff = (u32)((lane >> 3) * (D_ * 2) + ((lane & 7) ^ (2 * (lane >> 4))) * 16);
  g2.soff = (u32)(((lane >> 1) & 7) * (D_ * 2) + (lane & 1) * 16);
  g2.xoff = lane * 16;
  const int kg2 = w >> 2, wm2 = (w >> 1) & 1, wn2 = (w & 1) ^ kg2;
  v4u hB[4];
  if (isconv) {
    const int cb = xcd * 8 + (idx - 24);
    if (w == 7) {
      const int c8 = idx - 24;
      u32 t[42];
#pragma unroll
      for (int i = 0; i < 14; ++i) {
        int e = (i * 8 + c8) * 64 + lane;
        e = e < 6912 ? e : 0;
        const int row = e / 12, r12 = e % 12, mtx = r12 / 6, l6 = r12 % 6;
        t[i] = *(const u32*)((const char*)(mtx ? Wu : Wg) + (size_t)row * (F_ * 2) + xcd * 1152 + (l6 >> 1) * 384 + (l6 & 1) * 128);
      }
#pragma unroll
      for (int i = 0; i < 7; ++i) {
        int e = (i * 8 + c8) * 64 + lane;
        e = e < 3456 ? e : 0;
        const int row = e / 6, r6 = e % 6, mtx = r6 / 3, l3 = r6 % 3;
        t[14 + i] = *(const u32*)((const char*)(mtx ? Wu : Wg) + (size_t)row * (F_ * 2) + xcd * 1152 + l3 * 384 + 256);
      }
#pragma unroll
      for (int i = 0; i < 21; ++i) {
        int e = (i * 8 + c8) * 64 + lane;
        e = e < 10368 ? e : 0;
        t[21 + i] = *(const u32*)((const char*)Wd + (size_t)ksl * (1152 * 1152) + (size_t)e * 128);
      }
      u32 a = 0;
#pragma unroll
      for (int i = 0; i < 42; ++i) a ^= t[i];
      asm volatile("" :: "v"(a));
    }
#pragma unroll 1
    for (int c = w; c < 9 && w < 7; c += 7) {
      conv_channel(smem + w * C_WAVE, cb * 9 + c, lane, ssmk, ssmD, ws);
    }
    stage_barrier();
#pragma unroll
    for (int i = 0; i < 4; ++i) hB[i] = v4u{0u, 0u, 0u, 0u};
    if (wn2 == 0) gemm2_body<5, false>(smem, sb, g2, w, lane, kg2, wm2, 4, tile2, ksl, xin, out, ws, nullptr, hB, 0u);
    else          gemm2_body<4, false>(smem, sb, g2, w, lane, kg2, wm2, 0, tile2, ksl, xin, out, ws, nullptr, hB, 0u);
  } else {
    const int tile_n = xcd * 3 + idx % 3, tile_m = idx / 3;
    const int wm = w & 3, wn = w >> 2;
    const srd_t sw = make_srd((w >> 2) ? Wu : Wg, D_ * F_ * 2);
    const srd_t sx = make_srd(ws + WS_XNT, S_ * D_ * 2);
    const u32 wsoff = (u32)((8 * (w & 3)) * (F_ * 2) + tile_n * 384);
    const u32 xsoff = (u32)((tile_m * 16 + 2 * w) * (18 * 1024));
    u32* hsigA = (u32*)(ws + WS_HCNT) + ((2 * tile_m + (wm >> 1)) * 4 + ksl) * 32 + (tile_n % 6) * 4 + (wm & 1) * 2 + wn;
    const u32* hflg = (const u32*)(ws + WS_HCNT) + ((tile2 >> 2) * 4 + ksl) * 32 + (lane < 24 ? lane : 0);
    const u32 hoff = (u32)(((tile_m * 16 + wm * 4) * 144 + tile_n * 6) * 1024);
    gemm1_body(sb, sw, sx, wsoff, xsoff, w, lane, wm, wn, ws, hoff + 2 * wn * 1024, hsigA, g2, hflg, hB);
    const u32 hBoff = hoff + (4 + wn) * 1024;
    if (wn2 == 0) gemm2_body<5, true>(smem, sb, g2, w, lane, kg2, wm2, 4, tile2, ksl, xin, out, ws, hsigA + 128 * 32, hB, hBoff);
    else          gemm2_body<4, true>(smem, sb, g2, w, lane, kg2, wm2, 0, tile2, ksl, xin, out, ws, hsigA + 128 * 32, hB, hBoff);
  }
}

extern "C" void kernel_launch(void* const* d_in, const int* in_sizes, int n_in, void* d_out, int out_size,
                              void* d_ws, size_t ws_size, hipStream_t stream) {
  const u16* q = (const u16*)d_in[0];
  const u16* k = (const u16*)d_in[1];
  const u16* v = (const u16*)d_in[2];
  const u16* shk = (const u16*)d_in[3];
  const u16* ssmk = (const u16*)d_in[4];
  const u16* ssmD = (const u16*)d_in[5];
  const u16* Wg = (const u16*)d_in[7];
  const u16* Wu = (const u16*)d_in[8];
  const u16* Wd = (const u16*)d_in[9];
  char* ws = (char*)d_ws;
  (void)hipFuncSetAttribute((const void*)fused_kernel, hipFuncAttributeMaxDynamicSharedMemorySize, H_LDS);
  pre_kernel<<<256 + 288, 256, 0, stream>>>(q, k, v, shk, ws);
  fused_kernel<<<256, 512, H_LDS, stream>>>(Wg, Wu, Wd, ssmk, ssmD, q, (u16*)d_out, ws);
}
```

```cpp
#include <hip/hip_runtime.h>
#include <hip/hip_bf16.h>
#include <stdint.h>
#include <utility>

typedef unsigned short u16;
typedef unsigned int u32;
typedef short v4s __attribute__((ext_vector_type(4)));
typedef short v8s __attribute__((ext_vector_type(8)));
typedef float v4f __attribute__((ext_vector_type(4)));
typedef float v2f __attribute__((ext_vector_type(2)));
typedef u32 v4u __attribute__((ext_vector_type(4)));
typedef u32 v2u __attribute__((ext_vector_type(2)));
typedef __bf16 v2bf __attribute__((ext_vector_type(2)));
typedef __attribute__((address_space(3))) v4s* lds_v4s_ptr;

constexpr int S_ = 2048, D_ = 576, F_ = 4608;

#define DI __device__ __forceinline__

template <int... I, class Fn>
DI void static_for_impl(std::integer_sequence<int, I...>, Fn&& f) { (f(std::integral_constant<int, I>{}), ...); }
template <int N, class Fn>
DI void static_for(Fn&& f) { static_for_impl(std::make_integer_sequence<int, N>{}, f); }

DI float bf_lo(u32 w) { return __uint_as_float(w << 16); }
DI float bf_hi(u32 w) { return __uint_as_float(w & 0xffff0000u); }
DI float rbf(float f) { return (float)(__bf16)f; }
DI u32 pack_bf(float lo, float hi) {
  v2f t = {lo, hi};
  v2bf b = __builtin_convertvector(t, v2bf);
  return __builtin_bit_cast(u32, b);
}

template <int CTRL> DI float dpp_f(float v) {
  return __builtin_bit_cast(float, __builtin_amdgcn_update_dpp(0, __builtin_bit_cast(int, v), CTRL, 0xf, 0xf, false));
}
DI float sum32(float v) {
  v += dpp_f<0x128>(v);
  v += dpp_f<0x124>(v);
  v += dpp_f<0x122>(v);
  v += dpp_f<0x121>(v);
  return v + __shfl_xor(v, 16);
}
constexpr size_t WS_XNT = 0;
constexpr size_t WS_STATS = WS_XNT + (size_t)S_ * D_ * 2;
constexpr size_t WS_KVT = WS_STATS + (size_t)S_ * 8;
constexpr size_t WS_YT = WS_KVT + (size_t)S_ * D_ * 2;
constexpr size_t WS_HT = WS_YT + (size_t)S_ * D_ * 2;
constexpr size_t WS_PART = WS_HT + (size_t)S_ * F_ * 2;
constexpr size_t WS_CNT = WS_PART + (size_t)64 * 4 * 8 * 4 * 3072;
constexpr size_t WS_HCNT = WS_CNT + 2048 * 64;
constexpr size_t WS_YCNT = WS_HCNT + 3 * 64 * 128;

__global__ __launch_bounds__(256) void pre_kernel(const u16* __restrict__ x, const u16* __restrict__ kin,
                                                  const u16* __restrict__ vin, const u16* __restrict__ shk,
                                                  char* __restrict__ ws) {
  const int lane = threadIdx.x & 63, w = threadIdx.x >> 6;
  if (blockIdx.x < 8) *(v4u*)(ws + WS_CNT + (size_t)(blockIdx.x * 256 + threadIdx.x) * 64) = v4u{0u, 0u, 0u, 0u};
  else if (blockIdx.x < 15) *(v4u*)(ws + WS_HCNT + (size_t)((blockIdx.x - 8) * 256 + threadIdx.x) * 16) = v4u{0u, 0u, 0u, 0u};
  if (blockIdx.x < 256) {
    const int c = lane & 31;
    const int row = blockIdx.x * 8 + w * 2 + (lane >> 5);
    const v4u* src = (const v4u*)(x + (size_t)row * D_);
    v4u d[3];
    d[0] = src[c]; d[1] = src[c + 32]; d[2] = src[64 + (c & 7)];
    const float lastw = (c < 8) ? 1.f : 0.f;
    float f[3][8];
#pragma unroll
    for (int i = 0; i < 3; ++i)
#pragma unroll
      for (int j = 0; j < 4; ++j) { f[i][2 * j] = bf_lo(d[i][j]); f[i][2 * j + 1] = bf_hi(d[i][j]); }
    float s0 = 0.f, s1 = 0.f, s2 = 0.f;
#pragma unroll
    for (int j = 0; j < 8; ++j) { s0 += f[0][j]; s1 += f[1][j]; s2 += f[2][j]; }
    const float mean = sum32(s0 + s1 + s2 * lastw) * (1.0f / D_);
    float q0 = 0.f, q1 = 0.f, q2 = 0.f;
#pragma unroll
    for (int j = 0; j < 8; ++j) {
      const float a = f[0][j] - mean, b = f[1][j] - mean, e = f[2][j] - mean;
      q0 += a * a; q1 += b * b; q2 += e * e;
    }
    const float var = sum32(q0 + q1 + q2 * lastw) * (1.0f / D_);
    const float mb = rbf(mean);
    const float sd = rbf(sqrtf(rbf(rbf(var) + 1e-5f)));
    const float rinv = 1.0f / sd;
    if (c == 0) ((v2f*)(ws + WS_STATS))[row] = v2f{mb, rinv};
    char* xnt = ws + WS_XNT;
    const int tb = row >> 4, m = row & 15;
    const int cc = c & 3, T = cc >> 1, q0i = 2 * (cc & 1);
#pragma unroll
    for (int i = 0; i < 3; ++i) {
      u32 o[4];
#pragma unroll
      for (int j = 0; j < 4; ++j) {
        const u32 dd = pack_bf(f[i][2 * j] - mb, f[i][2 * j + 1] - mb);
        o[j] = pack_bf(bf_lo(dd) * rinv, bf_hi(dd) * rinv);
      }
      const int kb = 8 * i + (c >> 2);
      char* base = xnt + ((size_t)(tb * 18 + kb) * 64 + (q0i * 16 + m)) * 16 + 8 * T;
      if (i < 2 || c < 8) {
        *(v2u*)(base) = v2u{o[0], o[1]};
        *(v2u*)(base + 256) = v2u{o[2], o[3]};
      }
    }
  } else {
    const int b = blockIdx.x - 256;
    const int tt = b / 9, ct = b % 9;
    const int tg = lane >> 5, cx = lane & 31;
    const int s0 = tt * 64 + w * 16 + tg * 8, d0 = ct * 64 + cx * 2;
    u32 kk[11], vv[8];
#pragma unroll
    for (int i = 0; i < 11; ++i) {
      const int s = s0 - 3 + i;
      const u32 t = *(const u32*)(kin + (size_t)(s < 0 ? 0 : s) * D_ + d0);
      kk[i] = (i < 3 && s < 0) ? 0u : t;
    }
#pragma unroll
    for (int i = 0; i < 8; ++i) vv[i] = *(const u32*)(vin + (size_t)(s0 + i) * D_ + d0);
    const v4u w0 = *(const v4u*)(shk + (size_t)d0 * 4);
    u16* kvt = (u16*)(ws + WS_KVT);
#pragma unroll
    for (int ch = 0; ch < 2; ++ch) {
      const u32 wa = w0[2 * ch], wb = w0[2 * ch + 1];
      const float t0 = bf_lo(wa), t1 = bf_hi(wa), t2 = bf_lo(wb), t3 = bf_hi(wb);
      float kf[11];
#pragma unroll
      for (int i = 0; i < 11; ++i) kf[i] = ch ? bf_hi(kk[i]) : bf_lo(kk[i]);
      float r[8];
#pragma unroll
      for (int i = 0; i < 8; i += 2) {
        u32 a = pack_bf(kf[i] * t3, kf[i + 1] * t3);
        u32 p = pack_bf(kf[i + 1] * t2, kf[i + 2] * t2);
        a = pack_bf(bf_lo(a) + bf_lo(p), bf_hi(a) + bf_hi(p));
        p = pack_bf(kf[i + 2] * t1, kf[i + 3] * t1);
        a = pack_bf(bf_lo(a) + bf_lo(p), bf_hi(a) + bf_hi(p));
        p = pack_bf(kf[i + 3] * t0, kf[i + 4] * t0);
        a = pack_bf(bf_lo(a) + bf_lo(p), bf_hi(a) + bf_hi(p));
        r[i] = bf_lo(a) * (ch ? bf_hi(vv[i]) : bf_lo(vv[i]));
        r[i + 1] = bf_hi(a) * (ch ? bf_hi(vv[i + 1]) : bf_lo(vv[i + 1]));
      }
      v4u o = {pack_bf(r[7], r[6]), pack_bf(r[5], r[4]), pack_bf(r[3], r[2]), pack_bf(r[1], r[0])};
      *(v4u*)(kvt + (size_t)(d0 + ch) * S_ + s0) = o;
    }
  }
}

constexpr int G_STAGE = 34816, G_NBUF = 4, G_LDS = G_STAGE * G_NBUF;
constexpr int G_XOFF = 18432;

typedef __attribute__((address_space(3))) v8s* lds_v8s_ptr;
typedef __attribute__((address_space(3))) char* lds_ptr;
DI v8s tr_frag(u32 p, int off_lo, int off_hi) {
  v4s a = __builtin_amdgcn_ds_read_tr16_b64_v4i16((lds_v4s_ptr)((lds_ptr)p + off_lo));
  v4s b = __builtin_amdgcn_ds_read_tr16_b64_v4i16((lds_v4s_ptr)((lds_ptr)p + off_hi));
  return __builtin_shufflevector(a, b, 0, 1, 2, 3, 4, 5, 6, 7);
}
DI v4s tr_half(u32 p, int off) { return __builtin_amdgcn_ds_read_tr16_b64_v4i16((lds_v4s_ptr)((lds_ptr)p + off)); }
DI v8s join(const v4s& a, const v4s& b) { return __builtin_shufflevector(a, b, 0, 1, 2, 3, 4, 5, 6, 7); }
DI v8s ld_frag(u32 p, int off) { return *(lds_v8s_ptr)((lds_ptr)p + off); }
DI u32 opaque(u32 v) { asm volatile("" : "+v"(v)); return v; }
DI u32 lds_addr(const void* p) { return (u32)(size_t)(lds_ptr)p; }
typedef __amdgpu_buffer_rsrc_t srd_t;
DI srd_t make_srd(const void* p, u32 bytes) {
  return __builtin_amdgcn_make_buffer_rsrc((void*)p, 0, bytes, 0x00020000);
}
DI void dma16(u32 m0v, u32 voff, srd_t srd, u32 soff) {
  asm volatile("s_mov_b32 m0, %0\n\ts_nop 0\n\tbuffer_load_dwordx4 %1, %2, %3 offen lds"
               :: "s"(m0v), "v"(voff), "s"(srd), "s"(soff) : "memory");
}
DI void dma16_sc1(u32 m0v, u32 voff, srd_t srd, u32 soff) {
  asm volatile("s_mov_b32 m0, %0\n\ts_nop 0\n\tbuffer_load_dwordx4 %1, %2, %3 offen sc1 lds"
               :: "s"(m0v), "v"(voff), "s"(srd), "s"(soff) : "memory");
}
DI void wait_count(u32* p, u32 target) {
  u32 spins = 0;
  while (__hip_atomic_load(p, __ATOMIC_RELAXED, __HIP_MEMORY_SCOPE_AGENT) < target) {
    __builtin_amdgcn_s_sleep(1);
    if (++spins > (1u << 24)) break;
  }
  __builtin_amdgcn_fence(__ATOMIC_ACQUIRE, "wavefront");
}
#define SCHED0() __builtin_amdgcn_sched_barrier(0)
DI void mfma16(v4f& c, const v8s& a, const v8s& b) {
  asm volatile("v_mfma_f32_16x16x32_bf16 %0, %1, %2, %0" : "+v"(c) : "v"(a), "v"(b));
}
DI void mfma16_first(v4f& c, const v8s& a, const v8s& b) {
  asm volatile("v_mfma_f32_16x16x32_bf16 %0, %1, %2, 0" : "=&v"(c) : "v"(a), "v"(b));
}
DI void mfma_drain() { asm volatile("s_nop 15\n\ts_nop 15" ::: "memory"); }
template <int N> DI void wait_vm() { asm volatile("s_waitcnt vmcnt(%0)" :: "n"(N) : "memory"); }
DI void stage_barrier() { asm volatile("s_waitcnt lgkmcnt(0)\n\ts_barrier" ::: "memory"); }

constexpr int C_KH = 1152 * 8, C_XBYTES = 8 * 768, C_WAVE = C_KH + C_XBYTES;
constexpr int C_SH = 8 * C_WAVE;
typedef u32 v3u __attribute__((ext_vector_type(3)));

DI v3u kern3(srd_t sk, int dw) {
  v3u t;
  t[0] = __builtin_amdgcn_raw_buffer_load_b32(sk, (u32)(dw * 4), 0, 0);
  asm volatile("" ::: "memory");
  t[1] = __builtin_amdgcn_raw_buffer_load_b32(sk, (u32)(dw * 4 + 4), 0, 0);
  asm volatile("" ::: "memory");
  t[2] = __builtin_amdgcn_raw_buffer_load_b32(sk, (u32)(dw * 4 + 8), 0, 0);
  return t;
}
DI v4u units2(const v3u& d) {
  return v4u{d[0], d[1], __builtin_amdgcn_alignbit(d[1], d[0], 16), __builtin_amdgcn_alignbit(d[2], d[1], 16)};
}
DI void y_store(srd_t sy, const char* xl, const v4f& a, int n, int ib, int i16, int q, float Dd) {
  const int beta = 16 * n + i16;
  const v2u kvp = *(const v2u*)(xl + (2 * ib + (q >> 1)) * 768 + (beta + 16) * 16 + 8 * (1 - (q & 1)));
  const float k3 = bf_lo(kvp[0]), k2 = bf_hi(kvp[0]), k1 = bf_lo(kvp[1]), k0 = bf_hi(kvp[1]);
  const float y0 = rbf(a[0]) + rbf(k0 * Dd), y1 = rbf(a[1]) + rbf(k1 * Dd);
  const float y2 = rbf(a[2]) + rbf(k2 * Dd), y3 = rbf(a[3]) + rbf(k3 * Dd);
  __builtin_amdgcn_raw_buffer_store_b64(v2u{pack_bf(y0, y1), pack_bf(y2, y3)}, sy, (u32)((64 * beta + 16 * ib + 4 * q) * 2), 0, 16);
}

DI void conv_wg(char* smem, int cb, int w, int lane, int xcd, int c8, const u16* __restrict__ ssmk,
                const u16* __restrict__ ssmD, const u16* __restrict__ Wg, const u16* __restrict__ Wu,
                const u16* __restrict__ Wd, char* __restrict__ ws) {
  const int ch = cb * 9 + w, ch9 = cb * 9 + 8;
  char* kl = smem + w * C_WAVE;
  char* xl = kl + C_KH;
  char* kl9 = smem + C_SH;
  char* xl9 = kl9 + 2080 * 8;
  const srd_t sk = make_srd(ssmk + (size_t)ch * S_, S_ * 2);
  const srd_t sk9 = make_srd(ssmk + (size_t)ch9 * S_, S_ * 2);
  const v4u* xs = (const v4u*)(ws + WS_KVT + (size_t)ch * S_ * 2);
  const u32 dw0 = ((const u32*)ssmD)[ch >> 1], dw9 = ((const u32*)ssmD)[ch9 >> 1];
  const float Dd0 = (ch & 1) ? bf_hi(dw0) : bf_lo(dw0), Dd9 = (ch9 & 1) ? bf_hi(dw9) : bf_lo(dw9);
  v4u xd[4];
#pragma unroll
  for (int it = 0; it < 4; ++it) xd[it] = xs[it * 64 + lane];
  v3u kd[9];
  kd[0] = kern3(sk, lane - 16);
#pragma unroll
  for (int it = 1; it < 9; ++it) kd[it] = __builtin_amdgcn_raw_buffer_load_b96(sk, (u32)((it * 64 + lane - 16) * 4), 0, 0);
  v3u k9[3];
#pragma unroll
  for (int it = 0; it < 3; ++it) k9[it] = kern3(sk9, w * 130 + it * 64 + lane - 16);
  const v4u x9 = ((const v4u*)(ws + WS_KVT + (size_t)ch9 * S_ * 2))[w * 32 + (lane & 31)];
  u32 pf[6];
  const int wv = c8 * 8 + w;
  auto warmA = [&]() {
#pragma unroll
    for (int i = 0; i < 2; ++i) {
      int e = (i * 64 + wv) * 64 + lane;
      e = e < 6912 ? e : 0;
      const int row = e / 12, r12 = e % 12, mtx = r12 / 6, l6 = r12 % 6;
      pf[i] = *(const u32*)((const char*)(mtx ? Wu : Wg) + (size_t)row * (F_ * 2) + xcd * 1152 + (l6 >> 1) * 384 + (l6 & 1) * 128);
    }
  };
  auto warmB = [&]() {
    {
      int e = wv * 64 + lane;
      e = e < 3456 ? e : 0;
      const int row = e / 6, r6 = e % 6, mtx = r6 / 3, l3 = r6 % 3;
      pf[2] = *(const u32*)((const char*)(mtx ? Wu : Wg) + (size_t)row * (F_ * 2) + xcd * 1152 + l3 * 384 + 256);
    }
#pragma unroll
    for (int i = 0; i < 3; ++i) {
      int e = (i * 64 + wv) * 64 + lane;
      e = e < 10368 ? e : 0;
      pf[3 + i] = *(const u32*)((const char*)Wd + (size_t)(xcd >> 1) * (1152 * 1152) + (size_t)e * 128);
    }
  };
  SCHED0();
  warmA();
  SCHED0();
#pragma unroll
  for (int it = 0; it < 2; ++it) {
    const int z = it * 64 + lane;
    *(v4u*)(xl + (z >> 4) * 768 + (z & 15) * 16) = v4u{0u, 0u, 0u, 0u};
  }
  if (lane < 16) *(v4u*)(xl9 + w * 768 + lane * 16) = v4u{0u, 0u, 0u, 0u};
#pragma unroll
  for (int it = 0; it < 4; ++it) {
    const int cidx = it * 64 + lane;
    *(v4u*)(xl + (cidx & 7) * 768 + ((cidx >> 3) + 16) * 16) = xd[it];
  }
#pragma unroll
  for (int it = 0; it < 9; ++it)
    *(v4u*)(kl + (it * 64 + lane) * 16) = units2(kd[it]);
#pragma unroll
  for (int it = 0; it < 3; ++it)
    if (it < 2 || lane < 2) *(v4u*)(kl9 + (w * 130 + it * 64 + lane) * 16) = units2(k9[it]);
  if (lane < 32) {
    const int cidx = w * 32 + lane;
    *(v4u*)(xl9 + (cidx & 7) * 768 + ((cidx >> 3) + 16) * 16) = x9;
  }
#pragma unroll
  for (int it = 0; it < 8; ++it) kd[it] = __builtin_amdgcn_raw_buffer_load_b96(sk, (u32)((512 + it * 64 + lane - 16) * 4), 0, 0);
  kd[8] = kern3(sk, 1024 + lane - 16);
  SCHED0();
  warmB();
  SCHED0();
  __builtin_amdgcn_s_waitcnt(0xC07F);
  __builtin_amdgcn_wave_barrier();

  __builtin_amdgcn_s_sleep(127);
  __builtin_amdgcn_s_sleep(40);
  const int i16 = lane & 15, q = lane >> 4;
  const u32 ap = opaque(lds_addr(kl) + (i16 - 8 * q - 7 + 32) * 8);
  const u32 bp = opaque(lds_addr(xl) + q * 768 + i16 * 16);
  v4f acc[4][2];
  v8s af[3][4], bfr[2][4];
  auto rdA = [&](int c, int j) -> v8s {
    const int delta = 64 * (c & 15) + 16 * j;
    v4s lo = *(volatile __attribute__((address_space(3))) v4s*)((lds_ptr)ap + 8 * delta);
    v4s hi = *(volatile __attribute__((address_space(3))) v4s*)((lds_ptr)ap + 8 * delta + 32);
    return __builtin_shufflevector(lo, hi, 0, 1, 2, 3, 4, 5, 6, 7);
  };
  auto rdB = [&](int c, int n, int kb) -> v8s { return ld_frag(bp, 4 * kb * 768 + 16 * (16 * n - c + 16)); };
#pragma unroll
  for (int j = 0; j < 4; ++j) af[0][j] = rdA(0, j);
#pragma unroll
  for (int j = 0; j < 4; ++j) bfr[0][j] = rdB(0, j >> 1, j & 1);
  SCHED0();
  static_for<32>([&](auto CC) {
    constexpr int c = decltype(CC)::value;
    if constexpr (c == 15) {
      __builtin_amdgcn_s_waitcnt(0xC07F);
#pragma unroll
      for (int it = 0; it < 9; ++it)
        *(v4u*)(kl + (it * 64 + lane) * 16) = units2(kd[it]);
      __builtin_amdgcn_s_waitcnt(0xC07F);
      __builtin_amdgcn_wave_barrier();
    }
    if constexpr (c + 1 < 32) {
#pragma unroll
      for (int j = 0; j < 4; ++j) af[(c + 1) % 3][j] = rdA(c + 1, j);
#pragma unroll
      for (int j = 0; j < 4; ++j)
        if (j >= 2 || c + 1 < 16) bfr[(c + 1) & 1][j] = rdB(c + 1, j >> 1, j & 1);
    }
    SCHED0();
#pragma unroll
    for (int n = 1; n >= 0; --n) {
      if (n == 0 && c >= 16) continue;
#pragma unroll
      for (int kb = 0; kb < 2; ++kb) {
#pragma unroll
        for (int ib = 0; ib < 4; ++ib) {
          const int j = ib - 2 * kb;
          if (c == 0 && j < 0) continue;
          const v8s& A = (j >= 0) ? af[c % 3][j >= 0 ? j : 0] : af[(c + 2) % 3][j < 0 ? j + 4 : 0];
          if (c == 0 && kb == 0) mfma16_first(acc[ib][n], A, bfr[c & 1][2 * n + kb]);
          else mfma16(acc[ib][n], A, bfr[c & 1][2 * n + kb]);
        }
      }
    }
    SCHED0();
  });
  mfma_drain();
  SCHED0();
  {
    const float Dd = Dd0;
    const srd_t sy = make_srd(ws + WS_YT + (size_t)ch * S_ * 2, S_ * 2);
#pragma unroll
    for (int n = 0; n < 2; ++n)
#pragma unroll
      for (int ib = 0; ib < 4; ++ib) y_store(sy, xl, acc[ib][n], n, ib, i16, q, Dd);
  }
  stage_barrier();
  {
    const int c0 = w < 6 ? 3 * w : w == 6 ? 20 : 26, c1 = w < 5 ? 3 * w + 3 : w == 5 ? 20 : w == 6 ? 26 : 32;
    const char* a9 = kl9 + (i16 - 8 * q - 7 + 32) * 8;
    const char* b9 = xl9 + q * 768 + (i16 + 16) * 16;
    v4f a2[4][2];
#pragma unroll
    for (int a = 0; a < 4; ++a) { a2[a][0] = v4f{0.f, 0.f, 0.f, 0.f}; a2[a][1] = v4f{0.f, 0.f, 0.f, 0.f}; }
#pragma unroll 1
    for (int c = c0; c < c1; ++c) {
      v8s win[6];
#pragma unroll
      for (int j = 0; j < 6; ++j) {
        const int delta = 64 * c + 16 * (j - 2);
        const int dd = delta < 0 ? 0 : delta;
        const v4s lo = *(const v4s*)(a9 + 8 * dd), hi = *(const v4s*)(a9 + 8 * dd + 32);
        win[j] = __builtin_shufflevector(lo, hi, 0, 1, 2, 3, 4, 5, 6, 7);
        if (j < 2 && c == 0) win[j] = v8s{0, 0, 0, 0, 0, 0, 0, 0};
      }
#pragma unroll
      for (int kb = 0; kb < 2; ++kb) {
        const v8s b1 = *(const v8s*)(b9 + 4 * kb * 768 + 16 * (16 - c));
#pragma unroll
        for (int ib = 0; ib < 4; ++ib)
          a2[ib][1] = __builtin_amdgcn_mfma_f32_16x16x32_bf16(win[2 + ib - 2 * kb], b1, a2[ib][1], 0, 0, 0);
      }
      if (c < 16) {
#pragma unroll
        for (int kb = 0; kb < 2; ++kb) {
          const v8s b0 = *(const v8s*)(b9 + 4 * kb * 768 - 16 * c);
#pragma unroll
          for (int ib = 0; ib < 4; ++ib)
            a2[ib][0] = __builtin_amdgcn_mfma_f32_16x16x32_bf16(win[2 + ib - 2 * kb], b0, a2[ib][0], 0, 0, 0);
        }
      }
    }
#pragma unroll
    for (int t = 0; t < 8; ++t) *(v4f*)(kl + t * 1024 + lane * 16) = a2[t & 3][t >> 2];
    stage_barrier();
    v4f sum = *(const v4f*)(smem + w * 1024 + lane * 16);
#pragma unroll
    for (int v = 1; v < 8; ++v) sum += *(const v4f*)(smem + v * C_WAVE + w * 1024 + lane * 16);
    const float Dd = Dd9;
    const srd_t sy = make_srd(ws + WS_YT + (size_t)ch9 * S_ * 2, S_ * 2);
    y_store(sy, xl9, sum, w >> 2, w & 3, i16, q, Dd);
  }
  u32 pa = 0;
#pragma unroll
  for (int i = 0; i < 6; ++i) pa ^= pf[i];
  asm volatile("" :: "v"(pa));
  asm volatile("s_waitcnt vmcnt(0)" ::: "memory");
  stage_barrier();
  if (w == 0 && lane == 0) __hip_atomic_fetch_add((u32*)(ws + WS_YCNT), 9u, __ATOMIC_RELAXED, __HIP_MEMORY_SCOPE_AGENT);
}

constexpr int H_LDS = 163840;
constexpr int A_STAGE = 32768;
constexpr int b_slot(int s) { return (s % 3) == 0 ? 65536 : (s % 3) == 1 ? 114688 : 0; }
constexpr int g_slot(int s) { return (s & 3) == 0 ? 49152 : (s & 3) == 1 ? 83968 : (s & 3) == 2 ? 118784 : 0; }
constexpr int gb_store(int st) { return st == 1 ? 2 : st == 2 ? 2 : (st >= 3 && st <= 6) ? 1 : 0; }
constexpr int gb_extra(int st) { return (st == 4 || st == 6) ? 1 : 0; }
constexpr int gb_wait(int st) {
  return st >= 7 ? 0 : (st > 0 ? gb_store(st - 1) + gb_extra(st - 1) : 0) + 6 + gb_store(st);
}

DI void h_signal(u32* p, int lane) {
  if (lane == 0) __hip_atomic_store(p, 1u, __ATOMIC_RELAXED, __HIP_MEMORY_SCOPE_AGENT);
}
DI void wait_flags(const u32* p) {
  u32 spins = 0;
  while (!__all(__hip_atomic_load(p, __ATOMIC_RELAXED, __HIP_MEMORY_SCOPE_AGENT) != 0u)) {
    __builtin_amdgcn_s_sleep(1);
    if (++spins > (1u << 24)) break;
  }
  __builtin_amdgcn_fence(__ATOMIC_ACQUIRE, "wavefront");
}
DI u32 h_dword(float g0, float g1, float u0, float u1) {
  const u32 g = pack_bf(g0, g1), u = pack_bf(u0, u1);
  return pack_bf(bf_lo(g) * bf_lo(u), bf_hi(g) * bf_hi(u));
}
DI void poll_issue(u32& v, const u32* p) {
  asm volatile("global_load_dword %0, %1, off sc1" : "=v"(v) : "v"(p) : "memory");
}
DI void poll_check(u32& v, u32* p, u32 target) {
  asm volatile("" : "+v"(v));
  if (__builtin_amdgcn_readfirstlane(v) < target) wait_count(p, target);
}

DI void flags_check(u32& v, const u32* p) {
  asm volatile("" : "+v"(v));
  if (!__all(v != 0u)) wait_flags(p);
}

struct G2Dma {
  srd_t sw, sx;
  u32 wsoffA, wsoffB, xsoff, m0w, m0x, woff, soff, xoff;
};
DI void g2_piece(const G2Dma& d, int lane, int st, int i) {
  const u32 bo = g_slot(st);
  const int kb = st < 6 ? 6 * st : st < 12 ? 6 * (st - 6) + 1 : 6 * (st - 12) + 4;
  const int kb2 = kb + (st < 12 ? 2 : 1);
  const u32 wo = (st < 12 ? d.wsoffA : d.wsoffB) + (u32)kb * (32 * D_ * 2);
  if (i == 0) dma16(d.m0w + bo, d.woff, d.sw, wo);
  else if (i == 1) dma16(d.m0w + bo + 1024, d.woff, d.sw, wo + 128);
  else if (i == 2) { if (lane < 16) dma16(d.m0w + bo + 2048, d.soff, d.sw, wo + 256); }
  else if (i == 3) dma16_sc1(d.m0x + bo, d.xoff, d.sx, d.xsoff + kb * 1024);
  else dma16_sc1(d.m0x + bo + 1024, d.xoff, d.sx, d.xsoff + kb2 * 1024);
}

DI void gemm1_body(u32 sb, srd_t sw, srd_t sx, u32 wsoff, u32 xsoff, int w, int lane, int wm, int wn,
                   char* ws, u32 hoffA, u32* hsig, const G2Dma& g2, const u32* hflg, v4u (&hB)[4]) {
  constexpr int NU = 8;
  const int g = lane >> 4, qq = (lane & 15) >> 2, p = lane & 3;
  const int rr = 4 * (g & 1) + qq;
  const int wl0 = rr * 128 + (2 * (rr >> 1)) * 16 + p * 8;
  const u32 woff = (u32)((lane >> 3) * (F_ * 2) + ((lane & 7) ^ (2 * (lane >> 4))) * 16);
  const u32 xoff = lane * 16;
  v8s wf[3], xf[2][4];
  const u32 m0wB = sb + (w & 3) * 2048 + (w >> 2) * 1024;
  const u32 m0xB = sb + 16384 + w * 4096;
  auto dmaB = [&](int st, int i) {
    const u32 bo = b_slot(st);
    if (i < 2) dma16(m0wB + bo + i * 8192, woff, sw, wsoff + (u32)st * (64 * F_ * 2) + 256 + i * (32 * F_ * 2));
    else dma16(m0xB + bo + (i - 2) * 1024, xoff, sx, xsoff + st * 2048 + ((i - 2) >> 1) * (18 * 1024) + ((i - 2) & 1) * 1024);
  };
  v4f accA[2][4][4];
  {
    u32 wp[4], xp;
#pragma unroll
    for (int t = 0; t < 4; ++t) wp[t] = opaque((sb + wl0 + (g >> 1) * 4096 + wn * 1024) ^ (t << 5));
    xp = opaque(sb + 16384 + wm * 4096 + lane * 16);
    const u32 m0w = sb + (w & 3) * 4096 + (w >> 2) * 2048;
    const u32 m0x = sb + 16384 + w * 2048;
    auto dma_piece = [&](int st, int i) {
      const u32 bo = (st & 3) * A_STAGE;
      if (i < 2) dma16(m0w + bo + i * 1024, woff, sw, wsoff + (u32)st * (32 * F_ * 2) + i * 128);
      else dma16(m0x + bo + (i - 2) * 1024, xoff, sx, xsoff + st * 1024 + (i - 2) * (18 * 1024));
    };
#pragma unroll
    for (int st = 0; st < 3; ++st)
#pragma unroll
      for (int i = 0; i < 4; ++i) dma_piece(st, i);
    wait_vm<8>();
    stage_barrier();
#pragma unroll
    for (int mb = 0; mb < 4; ++mb) xf[0][mb] = ld_frag(xp, mb * 1024);
    wf[0] = tr_frag(wp[0], 0, 8192);
    wf[1] = tr_frag(wp[0], 2048, 2048 + 8192);
    dma_piece(3, 0); dma_piece(3, 1);
    SCHED0();
    static_for<18 * NU>([&](auto UC) {
      constexpr int U = decltype(UC)::value, st = U / NU, u = U % NU;
      constexpr int T = u >> 1, gu = u & 1;
      constexpr bool trans = (u == NU - 2 && st + 1 < 18);
      constexpr bool rd = (u + 2 < NU) || (st + 1 < 18);
      constexpr int un = (u + 2) % NU;
      auto mm = [&](int mb) {
        if constexpr (st == 0) mfma16_first(accA[gu][T][mb], wf[U % 3], xf[st & 1][mb]);
        else mfma16(accA[gu][T][mb], wf[U % 3], xf[st & 1][mb]);
        SCHED0();
      };
      v4s lo, hi;
      mm(0);
      if constexpr (!trans && rd) { lo = tr_half(wp[un >> 1], (un & 1) * 2048); SCHED0(); }
      mm(1);
      if constexpr (trans) {
        if constexpr (st + 1 <= 15) wait_vm<8>(); else if constexpr (st + 1 == 16) wait_vm<4>(); else wait_vm<8>();
        stage_barrier();
        constexpr int dl = (((st + 1) & 3) == 0) ? -3 * A_STAGE : A_STAGE;
#pragma unroll
        for (int t = 0; t < 4; ++t) wp[t] = opaque(wp[t] + dl);
        xp = opaque(xp + dl);
        lo = tr_half(wp[un >> 1], (un & 1) * 2048);
      }
      if constexpr (rd) { hi = tr_half(wp[un >> 1], (un & 1) * 2048 + 8192); wf[(U + 2) % 3] = join(lo, hi); }
      SCHED0();
      mm(2);
      if constexpr (u >= NU - 2 && st + 1 < 18) {
        xf[(st + 1) & 1][2 * (u - 6)] = ld_frag(xp, (2 * (u - 6)) * 1024);
        xf[(st + 1) & 1][2 * (u - 6) + 1] = ld_frag(xp, (2 * (u - 6) + 1) * 1024);
      }
      SCHED0();
      mm(3);
      if constexpr (u >= NU - 2) { if constexpr (st + 4 < 18) dma_piece(st + 4, u - (NU - 2)); }
      else if constexpr (u < 2) { if constexpr (st + 3 < 18) dma_piece(st + 3, u + 2); }
      constexpr int qB = U - (15 * NU + 6);
      if constexpr (qB >= 0 && qB < 12) dmaB(qB / 6, qB % 6);
      SCHED0();
    });
  }
  {
    v4f acc[2][2][4];
    v4u hA[8];
    u32 wp[2], xp, pollA = 0;
#pragma unroll
    for (int t = 0; t < 2; ++t) wp[t] = opaque((sb + b_slot(0) + wl0 + (g >> 1) * 2048) ^ ((2 * wn + t) << 5));
    xp = opaque(sb + b_slot(0) + 16384 + wm * 8192 + lane * 16);
    auto storeA = [&](int i) {
      __builtin_amdgcn_raw_buffer_store_b128(hA[i], g2.sx, lane * 16, hoffA + (i & 3) * (144 * 1024) + (i >> 2) * 1024, 16);
    };
    auto conv1 = [&](int cq) {
      const int gi = cq >> 2, pr = gi >> 2, mb = gi & 3, cT = 2 * pr + ((cq >> 1) & 1), ch = cq & 1;
      hA[gi][cq & 3] = h_dword(accA[0][cT][mb][2 * ch], accA[0][cT][mb][2 * ch + 1], accA[1][cT][mb][2 * ch], accA[1][cT][mb][2 * ch + 1]);
    };
    wait_vm<6>();
    stage_barrier();
#pragma unroll
    for (int mb = 0; mb < 4; ++mb) xf[0][mb] = ld_frag(xp, mb * 2048);
    wf[0] = tr_frag(wp[0], 0, 4096);
    wf[1] = tr_frag(wp[0], 1024, 1024 + 4096);
    dmaB(2, 0); dmaB(2, 1);
    mfma_drain();
    SCHED0();
    static_for<9 * NU>([&](auto UC) {
      constexpr int U = decltype(UC)::value, st = U / NU, u = U % NU;
      constexpr int T = (u >> 1) & 1, gu = u & 1;
      constexpr bool trans = (u == NU - 2 && st + 1 < 9);
      constexpr bool rd = (u + 2 < NU) || (st + 1 < 9);
      constexpr int un = (u + 2) % NU;
      constexpr int wo = (un >> 2) * 8192 + (un & 1) * 1024;
      auto mm = [&](int mb) {
        if constexpr (st == 0 && u < 4) mfma16_first(acc[gu][T][mb], wf[U % 3], xf[u >> 2][mb]);
        else mfma16(acc[gu][T][mb], wf[U % 3], xf[u >> 2][mb]);
        SCHED0();
      };
      v4s lo, hi;
      mm(0);
      if constexpr (!trans && rd) { lo = tr_half(wp[(un >> 1) & 1], wo); }
      if constexpr (U < 16) conv1(2 * U);
      SCHED0();
      mm(1);
      if constexpr (trans) {
        wait_vm<gb_wait(st)>();
        stage_barrier();
        if constexpr (st == 4) h_signal(hsig, lane);
        if constexpr (st == 6) poll_issue(pollA, hflg);
        if constexpr (st == 7) { h_signal(hsig + 64 * 32, lane); flags_check(pollA, hflg); }
        constexpr int dl = b_slot(st + 1) - b_slot(st);
#pragma unroll
        for (int t = 0; t < 2; ++t) wp[t] = opaque(wp[t] + dl);
        xp = opaque(xp + dl);
        lo = tr_half(wp[(un >> 1) & 1], wo);
      }
      if constexpr (rd) { hi = tr_half(wp[(un >> 1) & 1], wo + 2 * 2048); wf[(U + 2) % 3] = join(lo, hi); }
      SCHED0();
      mm(2);
      if constexpr (u == 1 || u == 2) { xf[1][2 * (u - 1)] = ld_frag(xp, (2 * (u - 1)) * 2048 + 1024); xf[1][2 * (u - 1) + 1] = ld_frag(xp, (2 * (u - 1) + 1) * 2048 + 1024); }
      if constexpr (u >= NU - 2 && st + 1 < 9) { xf[0][2 * (u - 6)] = ld_frag(xp, (2 * (u - 6)) * 2048); xf[0][2 * (u - 6) + 1] = ld_frag(xp, (2 * (u - 6) + 1) * 2048); }
      if constexpr (U < 16) conv1(2 * U + 1);
      SCHED0();
      mm(3);
      if constexpr (u >= NU - 2) { if constexpr (st + 3 < 9) dmaB(st + 3, u - (NU - 2)); }
      else if constexpr (u < 4) { if constexpr (st + 2 < 9) dmaB(st + 2, u + 2); }
      if constexpr ((st == 1 || st == 2) && (u == 4 || u == 5)) storeA(2 * (st - 1) + u - 4);
      if constexpr (st >= 3 && st <= 6 && u == 4) storeA(st + 1);
      constexpr int qG = U - (7 * NU + 6);
      if constexpr (qG >= 0 && qG < 4) { g2_piece(g2, lane, (2 * qG) / 3, (2 * qG) % 3); g2_piece(g2, lane, (2 * qG + 1) / 3, (2 * qG + 1) % 3); }
      if constexpr (qG == 4) g2_piece(g2, lane, 2, 2);
      if constexpr (qG >= 5 && qG < 8) { g2_piece(g2, lane, qG - 5, 3); g2_piece(g2, lane, qG - 5, 4); }
      SCHED0();
    });
    mfma_drain();
    SCHED0();
#pragma unroll
    for (int mb = 0; mb < 4; ++mb)
#pragma unroll
      for (int c = 0; c < 4; ++c)
        hB[mb][c] = h_dword(acc[0][c >> 1][mb][2 * (c & 1)], acc[0][c >> 1][mb][2 * (c & 1) + 1],
                            acc[1][c >> 1][mb][2 * (c & 1)], acc[1][c >> 1][mb][2 * (c & 1) + 1]);
  }
}

template <int NT, bool PRE>
DI void gemm2_body(char* smem, u32 sb, const G2Dma& g2, int w, int lane, int kg, int wm, int tbase, int tile, int ksl,
                   const u16* __restrict__ xin, u16* __restrict__ out, char* ws, u32* hsigB, const v4u (&hB)[4],
                   u32 hBoff) {
  constexpr int NU = NT;
  constexpr int NO = (2 * NT + 3) / 4;
  v4f acc[NT][4];
  const int g = lane >> 4, qq = (lane & 15) >> 2, p = lane & 3;
  const int rr = 4 * (g & 1) + qq;
  const int wl0 = rr * 128 + (2 * (rr >> 1)) * 16 + p * 8;
  u32 wp[NT], xp;
#pragma unroll
  for (int t = 0; t < 4; ++t)
    wp[t] = opaque((sb + g_slot(0) + wl0 + ((g >> 1) + 4 * kg) * 2304 + (NT == 5 ? 1024 : 0)) ^ (t << 5));
  if constexpr (NT == 5) wp[4] = opaque(sb + g_slot(0) + 2048 + rr * 32 + p * 8 + ((g >> 1) + 4 * kg) * 2304);
  xp = opaque(sb + g_slot(0) + G_XOFF + wm * 8192 + kg * 1024 + lane * 16);
  auto rdW = [&](int u) -> v8s { return tr_frag(wp[u], 0, 2 * 2304); };
  const u32* hflg = (const u32*)(ws + WS_HCNT) + ((tile >> 2) * 4 + ksl) * 32 + (lane < 24 ? lane : 0);
  u32* ycnt = (u32*)(ws + WS_YCNT);
  v8s wf[3], xf[2][4];
  u32 pollB = 0, pollY = 0;
  const int j = 2 * wm + kg;
  const int tok0 = (tile >> 2) * 128 + j * 32 + (lane & 15) + 16 * (ksl & 1);
  const int dcol = (tile & 3) * 144 + tbase * 16 + (lane >> 4) * 4 + 16 * (ksl >> 1);
  v2u xv[NO]; u32 yv[NO][4]; v2f stt;
  constexpr int NL = 1 + 5 * NO;
  char* xw = smem + ((wm * 2 + kg) * 9 + tbase) * 2048 + lane * 16;
  if constexpr (!PRE) {
#pragma unroll
    for (int st = 0; st < 3; ++st)
#pragma unroll
      for (int i = 0; i < 3; ++i) g2_piece(g2, lane, st, i);
    wait_flags(hflg);
#pragma unroll
    for (int st = 0; st < 3; ++st) { g2_piece(g2, lane, st, 3); g2_piece(g2, lane, st, 4); }
  }
  wait_vm<4>();
  stage_barrier();
#pragma unroll
  for (int mb = 0; mb < 4; ++mb) xf[0][mb] = ld_frag(xp, mb * 2048);
  wf[0] = rdW(0);
  wf[1] = rdW(1);
  g2_piece(g2, lane, 3, 0); g2_piece(g2, lane, 3, 1);
  SCHED0();
  static_for<18 * NU>([&](auto UC) {
    constexpr int U = decltype(UC)::value, st = U / NU, u = U % NU;
    constexpr int SB = PRE ? 2 : 0;
    constexpr bool trans = (u == NU - 2 && st + 1 < 18);
    constexpr bool rd = (u + 2 < NU) || (st + 1 < 18);
    constexpr int un = (u + 2) % NU;
    auto mm = [&](int mb) {
      if constexpr (st == 0) mfma16_first(acc[u][mb], wf[U % 3], xf[st & 1][mb]);
      else mfma16(acc[u][mb], wf[U % 3], xf[st & 1][mb]);
      SCHED0();
    };
    v4s lo, hi;
    mm(0);
    if constexpr (!trans && rd) { lo = tr_half(wp[un], 0); SCHED0(); }
    mm(1);
    if constexpr (trans) {
      constexpr int NW = st == 0 ? 7 + SB : st == 1 ? 11 + 2 * SB : st == 2 ? 10 + SB : st == 3 ? 10 + SB
                         : st + 1 <= 15 ? 10 : st + 1 == 16 ? 5 : 0;
      wait_vm<NW>();
      stage_barrier();
      if constexpr (st == 0) poll_issue(pollB, hflg + 64 * 32);
      if constexpr (st == 2) flags_check(pollB, hflg + 64 * 32);
      if constexpr (PRE && st == 4) h_signal(hsigB, lane);
      if constexpr (st == 6) poll_issue(pollB, hflg + 128 * 32);
      if constexpr (st == 8) flags_check(pollB, hflg + 128 * 32);
      if constexpr (st == 12) poll_issue(pollY, ycnt);
      constexpr int dl = g_slot(st + 1) - g_slot(st);
#pragma unroll
      for (int t = 0; t < NT; ++t) wp[t] = opaque(wp[t] + dl);
      xp = opaque(xp + dl);
      lo = tr_half(wp[un], 0);
    }
    if constexpr (rd) { hi = tr_half(wp[un], 2 * 2304); wf[(U + 2) % 3] = join(lo, hi); }
    SCHED0();
    mm(2);
    if constexpr (u >= NU - 2 && st + 1 < 18) {
      xf[(st + 1) & 1][2 * (u - (NU - 2))] = ld_frag(xp, (2 * (u - (NU - 2))) * 2048);
      xf[(st + 1) & 1][2 * (u - (NU - 2)) + 1] = ld_frag(xp, (2 * (u - (NU - 2)) + 1) * 2048);
    }
    if constexpr (u == 0) { if constexpr (st + 3 < 18) g2_piece(g2, lane, st + 3, 2); }
    SCHED0();
    mm(3);
    if constexpr (u >= NU - 2) { if constexpr (st + 4 < 18) g2_piece(g2, lane, st + 4, u - (NU - 2)); }
    else if constexpr (u == 0) { if constexpr (st + 3 < 18) g2_piece(g2, lane, st + 3, 3); }
    else if constexpr (u == 1) { if constexpr (st + 3 < 18) g2_piece(g2, lane, st + 3, 4); }
    if constexpr (PRE && u == 1 && st < 2) {
#pragma unroll
      for (int mb = 2 * st; mb < 2 * st + 2; ++mb)
        __builtin_amdgcn_raw_buffer_store_b128(hB[mb], g2.sx, lane * 16, hBoff + mb * (144 * 1024), 16);
    }
    if constexpr (st == 17 && u >= 1) {
#pragma unroll
      for (int b2 = 0; b2 < 2; ++b2) *(v4f*)(xw + ((u - 1) * 2 + b2) * 1024) = kg ? acc[u - 1][b2] : acc[u - 1][2 + b2];
    }
    SCHED0();
  });
  mfma_drain();
  SCHED0();
#pragma unroll
  for (int b2 = 0; b2 < 2; ++b2) *(v4f*)(xw + ((NT - 1) * 2 + b2) * 1024) = kg ? acc[NT - 1][b2] : acc[NT - 1][2 + b2];
  stage_barrier();
  v4f r[2 * NT];
  {
    const char* rd = smem + ((wm * 2 + (kg ^ 1)) * 9 + tbase) * 2048 + lane * 16;
#pragma unroll
    for (int T = 0; T < NT; ++T)
#pragma unroll
      for (int b2 = 0; b2 < 2; ++b2) {
        const v4f o = *(const v4f*)(rd + (T * 2 + b2) * 1024);
        r[T * 2 + b2] = kg ? (o + acc[T][2 + b2]) : (acc[T][b2] + o);
      }
  }
  const srd_t sp = make_srd(ws + WS_PART, 64 * 4 * 8 * 4 * 3072);
#pragma unroll
  for (int f = 0; f < 2 * NT; ++f)
    if ((f & 3) != ksl)
      __builtin_amdgcn_raw_buffer_store_b128(__builtin_bit_cast(v4u, r[f]), sp, lane * 16,
                                             (u32)((((tile * 4 + (f & 3)) * 8 + w) * 4 + ksl) * 3072 + (f >> 2) * 1024), 16);
  v4f own[NO];
#pragma unroll
  for (int i = 0; i < NO; ++i) {
    const v4f a0 = r[4 * i], a1 = r[4 * i + 1];
    const v4f a2 = (4 * i + 2 < 2 * NT) ? r[(4 * i + 2 < 2 * NT) ? 4 * i + 2 : 0] : a0;
    const v4f a3 = (4 * i + 3 < 2 * NT) ? r[(4 * i + 3 < 2 * NT) ? 4 * i + 3 : 0] : a0;
    own[i] = ksl == 0 ? a0 : ksl == 1 ? a1 : ksl == 2 ? a2 : a3;
  }
  SCHED0();
  poll_check(pollY, ycnt, 576u);
  {
    const srd_t sy = make_srd(ws + WS_YT, S_ * D_ * 2);
    stt = ((const v2f*)(ws + WS_STATS))[tok0];
#pragma unroll
    for (int i = 0; i < NO; ++i) {
      const int dc = (4 * i + ksl < 2 * NT) ? dcol + 32 * i : dcol;
      xv[i] = *(const v2u*)(xin + (size_t)tok0 * D_ + dc);
#pragma unroll
      for (int r = 0; r < 4; ++r) yv[i][r] = __builtin_amdgcn_raw_buffer_load_b16(sy, (u32)(((dc + r) * S_ + tok0) * 2), 0, 16);
    }
  }
  SCHED0();
  wait_vm<NL>();
  u32* flags = (u32*)(ws + WS_CNT);
  if (lane < 4 && lane != ksl)
    __hip_atomic_store(flags + ((tile * 4 + lane) * 8 + w) * 16 + ksl, 1u, __ATOMIC_RELAXED, __HIP_MEMORY_SCOPE_AGENT);
  float xa[NO][4];
#pragma unroll
  for (int i = 0; i < NO; ++i)
#pragma unroll
    for (int e = 0; e < 4; ++e) {
      const float xf_ = (e & 1) ? bf_hi(xv[i][e >> 1]) : bf_lo(xv[i][e >> 1]);
      const float xn = rbf(rbf(xf_ - stt[0]) * stt[1]);
      xa[i][e] = rbf(xf_ + rbf(xn * bf_lo(yv[i][e])));
    }
  {
    u32* fl = flags + ((tile * 4 + ksl) * 8 + w) * 16;
    u32 spins = 0;
    for (;;) {
      const u32 v = (lane < 4 && lane != ksl) ? __hip_atomic_load(fl + lane, __ATOMIC_RELAXED, __HIP_MEMORY_SCOPE_AGENT) : 1u;
      if (__all(v != 0u)) break;
      __builtin_amdgcn_s_sleep(1);
      if (++spins > (1u << 24)) break;
    }
  }
  __builtin_amdgcn_fence(__ATOMIC_ACQUIRE, "wavefront");
  v4f pp[3][NO];
#pragma unroll
  for (int i3 = 0; i3 < 3; ++i3) {
    const int src = i3 + (i3 >= ksl ? 1 : 0);
    const u32 po = (u32)((((tile * 4 + ksl) * 8 + w) * 4 + src) * 3072);
#pragma unroll
    for (int i = 0; i < NO; ++i)
      pp[i3][i] = __builtin_bit_cast(v4f, __builtin_amdgcn_raw_buffer_load_b128(sp, lane * 16, po + i * 1024, 16));
  }
#pragma unroll
  for (int i = 0; i < NO; ++i) {
    const v4f s0 = ksl == 0 ? own[i] : pp[0][i];
    const v4f s1 = ksl == 1 ? own[i] : (ksl < 1 ? pp[0][i] : pp[1][i]);
    const v4f s2 = ksl == 2 ? own[i] : (ksl < 2 ? pp[1][i] : pp[2][i]);
    const v4f s3 = ksl == 3 ? own[i] : pp[2][i];
    const v4f B = (s0 + s1) + (s2 + s3);
    const float o0 = xa[i][0] + rbf(B[0]), o1 = xa[i][1] + rbf(B[1]);
    const float o2 = xa[i][2] + rbf(B[2]), o3 = xa[i][3] + rbf(B[3]);
    if (4 * i + ksl < 2 * NT)
      *(v2u*)(out + (size_t)tok0 * D_ + dcol + 32 * i) = v2u{pack_bf(o0, o1), pack_bf(o2, o3)};
  }
}

__global__ __launch_bounds__(512, 2) void fused_kernel(const u16* __restrict__ Wg, const u16* __restrict__ Wu,
                                                       const u16* __restrict__ Wd, const u16* __restrict__ ssmk,
                                                       const u16* __restrict__ ssmD, const u16* __restrict__ xin,
                                                       u16* __restrict__ out, char* __restrict__ ws) {
  extern __shared__ __attribute__((aligned(1024))) char smem[];
  const int bid = blockIdx.x;
  const int xcd = bid & 7, idx = bid >> 3;
  const int tid = threadIdx.x, lane = tid & 63;
  const int w = __builtin_amdgcn_readfirstlane(tid >> 6);
  const int ksl = xcd >> 1;
  const bool isconv = idx >= 24;
  const int tile2 = isconv ? (2 * (idx - 24) + (xcd & 1)) * 4 + 3 : (2 * (idx / 3) + (xcd & 1)) * 4 + idx % 3;
  const u32 sb = lds_addr(smem);
  G2Dma g2;
  g2.sw = make_srd(Wd, D_ * F_ * 2);
  g2.sx = make_srd(ws + WS_HT, S_ * F_ * 2);
  g2.wsoffA = (u32)((ksl * 1152 + 8 * (w & 3) + (w >> 2) * 64) * (D_ * 2) + (tile2 & 3) * 288);
  g2.wsoffB = (u32)((ksl * 1152 + 8 * w) * (D_ * 2) + (tile2 & 3) * 288);
  g2.xsoff = (u32)((((tile2 >> 2) * 8 + w) * 144 + ksl * 36) * 1024);
  g2.m0w = sb + w * 2304;
  g2.m0x = sb + G_XOFF + w * 2048;
  g2.woff = (u32)((lane >> 3) * (D_ * 2) + ((lane & 7) ^ (2 * (lane >> 4))) * 16);
  g2.soff = (u32)(((lane >> 1) & 7) * (D_ * 2) + (lane & 1) * 16);
  g2.xoff = lane * 16;
  const int kg2 = w >> 2, wm2 = (w >> 1) & 1, wn2 = (w & 1) ^ kg2;
  v4u hB[4];
  if (isconv) {
    const int cb = xcd * 8 + (idx - 24);
    conv_wg(smem, cb, w, lane, xcd, idx - 24, ssmk, ssmD, Wg, Wu, Wd, ws);
#pragma unroll
    for (int i = 0; i < 4; ++i) hB[i] = v4u{0u, 0u, 0u, 0u};
    if (wn2 == 0) gemm2_body<5, false>(smem, sb, g2, w, lane, kg2, wm2, 4, tile2, ksl, xin, out, ws, nullptr, hB, 0u);
    else          gemm2_body<4, false>(smem, sb, g2, w, lane, kg2, wm2, 0, tile2, ksl, xin, out, ws, nullptr, hB, 0u);
  } else {
    const int tile_n = xcd * 3 + idx % 3, tile_m = idx / 3;
    const int wm = w & 3, wn = w >> 2;
    const srd_t sw = make_srd((w >> 2) ? Wu : Wg, D_ * F_ * 2);
    const srd_t sx = make_srd(ws + WS_XNT, S_ * D_ * 2);
    const u32 wsoff = (u32)((8 * (w & 3)) * (F_ * 2) + tile_n * 384);
    const u32 xsoff = (u32)((tile_m * 16 + 2 * w) * (18 * 1024));
    u32* hsigA = (u32*)(ws + WS_HCNT) + ((2 * tile_m + (wm >> 1)) * 4 + ksl) * 32 + (tile_n % 6) * 4 + (wm & 1) * 2 + wn;
    const u32* hflg = (const u32*)(ws + WS_HCNT) + ((tile2 >> 2) * 4 + ksl) * 32 + (lane < 24 ? lane : 0);
    const u32 hoff = (u32)(((tile_m * 16 + wm * 4) * 144 + tile_n * 6) * 1024);
    gemm1_body(sb, sw, sx, wsoff, xsoff, w, lane, wm, wn, ws, hoff + 2 * wn * 1024, hsigA, g2, hflg, hB);
    const u32 hBoff = hoff + (4 + wn) * 1024;
    if (wn2 == 0) gemm2_body<5, true>(smem, sb, g2, w, lane, kg2, wm2, 4, tile2, ksl, xin, out, ws, hsigA + 128 * 32, hB, hBoff);
    else          gemm2_body<4, true>(smem, sb, g2, w, lane, kg2, wm2, 0, tile2, ksl, xin, out, ws, hsigA + 128 * 32, hB, hBoff);
  }
}

extern "C" void kernel_launch(void* const* d_in, const int* in_sizes, int n_in, void* d_out, int out_size,
                              void* d_ws, size_t ws_size, hipStream_t stream) {
  const u16* q = (const u16*)d_in[0];
  const u16* k = (const u16*)d_in[1];
  const u16* v = (const u16*)d_in[2];
  const u16* shk = (const u16*)d_in[3];
  const u16* ssmk = (const u16*)d_in[4];
  const u16* ssmD = (const u16*)d_in[5];
  const u16* Wg = (const u16*)d_in[7];
  const u16* Wu = (const u16*)d_in[8];
  const u16* Wd = (const u16*)d_in[9];
  char* ws = (char*)d_ws;
  (void)hipFuncSetAttribute((const void*)fused_kernel, hipFuncAttributeMaxDynamicSharedMemorySize, H_LDS);
  pre_kernel<<<256 + 288, 256, 0, stream>>>(q, k, v, shk, ws);
  fused_kernel<<<256, 512, H_LDS, stream>>>(Wg, Wu, Wd, ssmk, ssmD, q, (u16*)d_out, ws);
}
```

```cpp
#include <hip/hip_runtime.h>
#include <hip/hip_bf16.h>
#include <stdint.h>
#include <utility>

typedef unsigned short u16;
typedef unsigned int u32;
typedef short v4s __attribute__((ext_vector_type(4)));
typedef short v8s __attribute__((ext_vector_type(8)));
typedef float v4f __attribute__((ext_vector_type(4)));
typedef float v2f __attribute__((ext_vector_type(2)));
typedef u32 v4u __attribute__((ext_vector_type(4)));
typedef u32 v2u __attribute__((ext_vector_type(2)));
typedef __bf16 v2bf __attribute__((ext_vector_type(2)));
typedef __attribute__((address_space(3))) v4s* lds_v4s_ptr;

constexpr int S_ = 2048, D_ = 576, F_ = 4608;

#define DI __device__ __forceinline__

template <int... I, class Fn>
DI void static_for_impl(std::integer_sequence<int, I...>, Fn&& f) { (f(std::integral_constant<int, I>{}), ...); }
template <int N, class Fn>
DI void static_for(Fn&& f) { static_for_impl(std::make_integer_sequence<int, N>{}, f); }

DI float bf_lo(u32 w) { return __uint_as_float(w << 16); }
DI float bf_hi(u32 w) { return __uint_as_float(w & 0xffff0000u); }
DI float rbf(float f) { return (float)(__bf16)f; }
DI u32 pack_bf(float lo, float hi) {
  v2f t = {lo, hi};
  v2bf b = __builtin_convertvector(t, v2bf);
  return __builtin_bit_cast(u32, b);
}

template <int CTRL> DI float dpp_f(float v) {
  return __builtin_bit_cast(float, __builtin_amdgcn_update_dpp(0, __builtin_bit_cast(int, v), CTRL, 0xf, 0xf, false));
}
DI float sum32(float v) {
  v += dpp_f<0x128>(v);
  v += dpp_f<0x124>(v);
  v += dpp_f<0x122>(v);
  v += dpp_f<0x121>(v);
  return v + __shfl_xor(v, 16);
}
constexpr size_t WS_XNT = 0;
constexpr size_t WS_STATS = WS_XNT + (size_t)S_ * D_ * 2;
constexpr size_t WS_KVT = WS_STATS + (size_t)S_ * 8;
constexpr size_t WS_YT = WS_KVT + (size_t)S_ * D_ * 2;
constexpr size_t WS_HT = WS_YT + (size_t)S_ * D_ * 2;
constexpr size_t WS_PART = WS_HT + (size_t)S_ * F_ * 2;
constexpr size_t WS_CNT = WS_PART + (size_t)64 * 4 * 8 * 4 * 3072;
constexpr size_t WS_HCNT = WS_CNT + 2048 * 64;
constexpr size_t WS_YCNT = WS_HCNT + 3 * 64 * 128;

__global__ __launch_bounds__(256) void pre_kernel(const u16* __restrict__ x, const u16* __restrict__ kin,
                                                  const u16* __restrict__ vin, const u16* __restrict__ shk,
                                                  char* __restrict__ ws) {
  const int lane = threadIdx.x & 63, w = threadIdx.x >> 6;
  if (blockIdx.x < 8) *(v4u*)(ws + WS_CNT + (size_t)(blockIdx.x * 256 + threadIdx.x) * 64) = v4u{0u, 0u, 0u, 0u};
  else if (blockIdx.x < 15) *(v4u*)(ws + WS_HCNT + (size_t)((blockIdx.x - 8) * 256 + threadIdx.x) * 16) = v4u{0u, 0u, 0u, 0u};
  if (blockIdx.x < 256) {
    const int c = lane & 31;
    const int row = blockIdx.x * 8 + w * 2 + (lane >> 5);
    const v4u* src = (const v4u*)(x + (size_t)row * D_);
    v4u d[3];
    d[0] = src[c]; d[1] = src[c + 32]; d[2] = src[64 + (c & 7)];
    const float lastw = (c < 8) ? 1.f : 0.f;
    float f[3][8];
#pragma unroll
    for (int i = 0; i < 3; ++i)
#pragma unroll
      for (int j = 0; j < 4; ++j) { f[i][2 * j] = bf_lo(d[i][j]); f[i][2 * j + 1] = bf_hi(d[i][j]); }
    float s0 = 0.f, s1 = 0.f, s2 = 0.f;
#pragma unroll
    for (int j = 0; j < 8; ++j) { s0 += f[0][j]; s1 += f[1][j]; s2 += f[2][j]; }
    const float mean = sum32(s0 + s1 + s2 * lastw) * (1.0f / D_);
    float q0 = 0.f, q1 = 0.f, q2 = 0.f;
#pragma unroll
    for (int j = 0; j < 8; ++j) {
      const float a = f[0][j] - mean, b = f[1][j] - mean, e = f[2][j] - mean;
      q0 += a * a; q1 += b * b; q2 += e * e;
    }
    const float var = sum32(q0 + q1 + q2 * lastw) * (1.0f / D_);
    const float mb = rbf(mean);
    const float sd = rbf(sqrtf(rbf(rbf(var) + 1e-5f)));
    const float rinv = 1.0f / sd;
    if (c == 0) ((v2f*)(ws + WS_STATS))[row] = v2f{mb, rinv};
    char* xnt = ws + WS_XNT;
    const int tb = row >> 4, m = row & 15;
    const int cc = c & 3, T = cc >> 1, q0i = 2 * (cc & 1);
#pragma unroll
    for (int i = 0; i < 3; ++i) {
      u32 o[4];
#pragma unroll
      for (int j = 0; j < 4; ++j) {
        const u32 dd = pack_bf(f[i][2 * j] - mb, f[i][2 * j + 1] - mb);
        o[j] = pack_bf(bf_lo(dd) * rinv, bf_hi(dd) * rinv);
      }
      const int kb = 8 * i + (c >> 2);
      char* base = xnt + ((size_t)(tb * 18 + kb) * 64 + (q0i * 16 + m)) * 16 + 8 * T;
      if (i < 2 || c < 8) {
        *(v2u*)(base) = v2u{o[0], o[1]};
        *(v2u*)(base + 256) = v2u{o[2], o[3]};
      }
    }
  } else {
    const int b = blockIdx.x - 256;
    const int tt = b / 9, ct = b % 9;
    const int tg = lane >> 5, cx = lane & 31;
    const int s0 = tt * 64 + w * 16 + tg * 8, d0 = ct * 64 + cx * 2;
    u32 kk[11], vv[8];
#pragma unroll
    for (int i = 0; i < 11; ++i) {
      const int s = s0 - 3 + i;
      const u32 t = *(const u32*)(kin + (size_t)(s < 0 ? 0 : s) * D_ + d0);
      kk[i] = (i < 3 && s < 0) ? 0u : t;
    }
#pragma unroll
    for (int i = 0; i < 8; ++i) vv[i] = *(const u32*)(vin + (size_t)(s0 + i) * D_ + d0);
    const v4u w0 = *(const v4u*)(shk + (size_t)d0 * 4);
    u16* kvt = (u16*)(ws + WS_KVT);
#pragma unroll
    for (int ch = 0; ch < 2; ++ch) {
      const u32 wa = w0[2 * ch], wb = w0[2 * ch + 1];
      const float t0 = bf_lo(wa), t1 = bf_hi(wa), t2 = bf_lo(wb), t3 = bf_hi(wb);
      float kf[11];
#pragma unroll
      for (int i = 0; i < 11; ++i) kf[i] = ch ? bf_hi(kk[i]) : bf_lo(kk[i]);
      float r[8];
#pragma unroll
      for (int i = 0; i < 8; i += 2) {
        u32 a = pack_bf(kf[i] * t3, kf[i + 1] * t3);
        u32 p = pack_bf(kf[i + 1] * t2, kf[i + 2] * t2);
        a = pack_bf(bf_lo(a) + bf_lo(p), bf_hi(a) + bf_hi(p));
        p = pack_bf(kf[i + 2] * t1, kf[i + 3] * t1);
        a = pack_bf(bf_lo(a) + bf_lo(p), bf_hi(a) + bf_hi(p));
        p = pack_bf(kf[i + 3] * t0, kf[i + 4] * t0);
        a = pack_bf(bf_lo(a) + bf_lo(p), bf_hi(a) + bf_hi(p));
        r[i] = bf_lo(a) * (ch ? bf_hi(vv[i]) : bf_lo(vv[i]));
        r[i + 1] = bf_hi(a) * (ch ? bf_hi(vv[i + 1]) : bf_lo(vv[i + 1]));
      }
      v4u o = {pack_bf(r[7], r[6]), pack_bf(r[5], r[4]), pack_bf(r[3], r[2]), pack_bf(r[1], r[0])};
      *(v4u*)(kvt + (size_t)(d0 + ch) * S_ + s0) = o;
    }
  }
}

constexpr int G_STAGE = 34816, G_NBUF = 4, G_LDS = G_STAGE * G_NBUF;
constexpr int G_XOFF = 18432;

typedef __attribute__((address_space(3))) v8s* lds_v8s_ptr;
typedef __attribute__((address_space(3))) char* lds_ptr;
DI v8s tr_frag(u32 p, int off_lo, int off_hi) {
  v4s a = __builtin_amdgcn_ds_read_tr16_b64_v4i16((lds_v4s_ptr)((lds_ptr)p + off_lo));
  v4s b = __builtin_amdgcn_ds_read_tr16_b64_v4i16((lds_v4s_ptr)((lds_ptr)p + off_hi));
  return __builtin_shufflevector(a, b, 0, 1, 2, 3, 4, 5, 6, 7);
}
DI v4s tr_half(u32 p, int off) { return __builtin_amdgcn_ds_read_tr16_b64_v4i16((lds_v4s_ptr)((lds_ptr)p + off)); }
DI v8s join(const v4s& a, const v4s& b) { return __builtin_shufflevector(a, b, 0, 1, 2, 3, 4, 5, 6, 7); }
DI v8s ld_frag(u32 p, int off) { return *(lds_v8s_ptr)((lds_ptr)p + off); }
DI u32 opaque(u32 v) { asm volatile("" : "+v"(v)); return v; }
DI u32 lds_addr(const void* p) { return (u32)(size_t)(lds_ptr)p; }
typedef __amdgpu_buffer_rsrc_t srd_t;
DI srd_t make_srd(const void* p, u32 bytes) {
  return __builtin_amdgcn_make_buffer_rsrc((void*)p, 0, bytes, 0x00020000);
}
DI void dma16(u32 m0v, u32 voff, srd_t srd, u32 soff) {
  asm volatile("s_mov_b32 m0, %0\n\ts_nop 0\n\tbuffer_load_dwordx4 %1, %2, %3 offen lds"
               :: "s"(m0v), "v"(voff), "s"(srd), "s"(soff) : "memory");
}
DI void dma16_sc1(u32 m0v, u32 voff, srd_t srd, u32 soff) {
  asm volatile("s_mov_b32 m0, %0\n\ts_nop 0\n\tbuffer_load_dwordx4 %1, %2, %3 offen sc1 lds"
               :: "s"(m0v), "v"(voff), "s"(srd), "s"(soff) : "memory");
}
DI void wait_count(u32* p, u32 target) {
  u32 spins = 0;
  while (__hip_atomic_load(p, __ATOMIC_RELAXED, __HIP_MEMORY_SCOPE_AGENT) < target) {
    __builtin_amdgcn_s_sleep(1);
    if (++spins > (1u << 24)) break;
  }
  __builtin_amdgcn_fence(__ATOMIC_ACQUIRE, "wavefront");
}
#define SCHED0() __builtin_amdgcn_sched_barrier(0)
DI void mfma16(v4f& c, const v8s& a, const v8s& b) {
  asm volatile("v_mfma_f32_16x16x32_bf16 %0, %1, %2, %0" : "+v"(c) : "v"(a), "v"(b));
}
DI void mfma16_first(v4f& c, const v8s& a, const v8s& b) {
  asm volatile("v_mfma_f32_16x16x32_bf16 %0, %1, %2, 0" : "=&v"(c) : "v"(a), "v"(b));
}
DI void mfma_drain() { asm volatile("s_nop 15\n\ts_nop 15" ::: "memory"); }
template <int N> DI void wait_vm() { asm volatile("s_waitcnt vmcnt(%0)" :: "n"(N) : "memory"); }
DI void stage_barrier() { asm volatile("s_waitcnt lgkmcnt(0)\n\ts_barrier" ::: "memory"); }

constexpr int C_KBYTES = 2080 * 8, C_XBYTES = 8 * 768, C_WAVE = C_KBYTES + C_XBYTES;

DI void conv_channel(char* kl, int ch, int lane, const u16* __restrict__ ssmk, const u16* __restrict__ ssmD,
                     char* __restrict__ ws) {
  char* xl = kl + C_KBYTES;
  const srd_t sk = make_srd(ssmk + (size_t)ch * S_, S_ * 2);
  const v4u* xs = (const v4u*)(ws + WS_KVT + (size_t)ch * S_ * 2);
  v4u xd[4];
#pragma unroll
  for (int it = 0; it < 4; ++it) xd[it] = xs[it * 64 + lane];
  u32 d0[17], d1[17], d2[17];
#pragma unroll
  for (int it = 0; it < 17; ++it) {
    const int dw = it * 64 + lane - 16;
    if (it == 0 || it == 16) {
      d0[it] = __builtin_amdgcn_raw_buffer_load_b32(sk, (u32)(dw * 4), 0, 0);
      asm volatile("" ::: "memory");
      d1[it] = __builtin_amdgcn_raw_buffer_load_b32(sk, (u32)(dw * 4 + 4), 0, 0);
      asm volatile("" ::: "memory");
      d2[it] = __builtin_amdgcn_raw_buffer_load_b32(sk, (u32)(dw * 4 + 8), 0, 0);
    } else {
      typedef u32 v3u __attribute__((ext_vector_type(3)));
      const v3u t = __builtin_amdgcn_raw_buffer_load_b96(sk, (u32)(dw * 4), 0, 0);
      d0[it] = t[0]; d1[it] = t[1]; d2[it] = t[2];
    }
  }
#pragma unroll
  for (int it = 0; it < 2; ++it) {
    const int z = it * 64 + lane;
    *(v4u*)(xl + (z >> 4) * 768 + (z & 15) * 16) = v4u{0u, 0u, 0u, 0u};
  }
#pragma unroll
  for (int it = 0; it < 4; ++it) {
    const int cidx = it * 64 + lane;
    *(v4u*)(xl + (cidx & 7) * 768 + ((cidx >> 3) + 16) * 16) = xd[it];
  }
#pragma unroll
  for (int it = 0; it < 17; ++it) {
    const int L = it * 64 + lane;
    v4u o = {d0[it], d1[it], __builtin_amdgcn_alignbit(d1[it], d0[it], 16), __builtin_amdgcn_alignbit(d2[it], d1[it], 16)};
    if (it < 16 || lane < 16) *(v4u*)(kl + L * 16) = o;
  }
  __builtin_amdgcn_s_waitcnt(0xC07F);
  __builtin_amdgcn_wave_barrier();

  const int i16 = lane & 15, q = lane >> 4;
  const u32 ap = opaque(lds_addr(kl) + (i16 - 8 * q - 7 + 32) * 8);
  const u32 bp = opaque(lds_addr(xl) + q * 768 + i16 * 16);
  v4f acc[4][2];
  v8s af[3][4], bfr[2][4];
  auto rdA = [&](int c, int j) -> v8s {
    const int delta = 64 * c + 16 * j;
    v4s lo = *(volatile __attribute__((address_space(3))) v4s*)((lds_ptr)ap + 8 * delta);
    v4s hi = *(volatile __attribute__((address_space(3))) v4s*)((lds_ptr)ap + 8 * delta + 32);
    return __builtin_shufflevector(lo, hi, 0, 1, 2, 3, 4, 5, 6, 7);
  };
  auto rdB = [&](int c, int n, int kb) -> v8s { return ld_frag(bp, 4 * kb * 768 + 16 * (16 * n - c + 16)); };
#pragma unroll
  for (int j = 0; j < 4; ++j) af[0][j] = rdA(0, j);
#pragma unroll
  for (int j = 0; j < 4; ++j) bfr[0][j] = rdB(0, j >> 1, j & 1);
  SCHED0();
  static_for<32>([&](auto CC) {
    constexpr int c = decltype(CC)::value;
    if constexpr (c + 1 < 32) {
#pragma unroll
      for (int j = 0; j < 4; ++j) af[(c + 1) % 3][j] = rdA(c + 1, j);
#pragma unroll
      for (int j = 0; j < 4; ++j)
        if (j >= 2 || c + 1 < 16) bfr[(c + 1) & 1][j] = rdB(c + 1, j >> 1, j & 1);
    }
    SCHED0();
#pragma unroll
    for (int n = 1; n >= 0; --n) {
      if (n == 0 && c >= 16) continue;
#pragma unroll
      for (int kb = 0; kb < 2; ++kb) {
#pragma unroll
        for (int ib = 0; ib < 4; ++ib) {
          const int j = ib - 2 * kb;
          if (c == 0 && j < 0) continue;
          const v8s& A = (j >= 0) ? af[c % 3][j >= 0 ? j : 0] : af[(c + 2) % 3][j < 0 ? j + 4 : 0];
          if (c == 0 && kb == 0) mfma16_first(acc[ib][n], A, bfr[c & 1][2 * n + kb]);
          else mfma16(acc[ib][n], A, bfr[c & 1][2 * n + kb]);
        }
      }
    }
    SCHED0();
  });
  mfma_drain();
  SCHED0();
  const float Dd = bf_lo((u32)ssmD[ch]);
  const srd_t sy = make_srd(ws + WS_YT + (size_t)ch * S_ * 2, S_ * 2);
#pragma unroll
  for (int n = 0; n < 2; ++n)
#pragma unroll
    for (int ib = 0; ib < 4; ++ib) {
      const int beta = 16 * n + i16;
      const v2u kvp = *(const v2u*)(xl + (2 * ib + (q >> 1)) * 768 + (beta + 16) * 16 + 8 * (1 - (q & 1)));
      const float k3 = bf_lo(kvp[0]), k2 = bf_hi(kvp[0]), k1 = bf_lo(kvp[1]), k0 = bf_hi(kvp[1]);
      const float y0 = rbf(acc[ib][n][0]) + rbf(k0 * Dd);
      const float y1 = rbf(acc[ib][n][1]) + rbf(k1 * Dd);
      const float y2 = rbf(acc[ib][n][2]) + rbf(k2 * Dd);
      const float y3 = rbf(acc[ib][n][3]) + rbf(k3 * Dd);
      __builtin_amdgcn_raw_buffer_store_b64(v2u{pack_bf(y0, y1), pack_bf(y2, y3)}, sy, (u32)((64 * beta + 16 * ib + 4 * q) * 2), 0, 16);
    }
  asm volatile("s_waitcnt vmcnt(0) lgkmcnt(0)" ::: "memory");
  if (lane == 0) __hip_atomic_fetch_add((u32*)(ws + WS_YCNT), 1u, __ATOMIC_RELAXED, __HIP_MEMORY_SCOPE_AGENT);
  __builtin_amdgcn_wave_barrier();
}

constexpr int H_LDS = 163840;
constexpr int A_STAGE = 32768;
constexpr int b_slot(int s) { return (s % 3) == 0 ? 65536 : (s % 3) == 1 ? 114688 : 0; }
constexpr int g_slot(int s) { return (s & 3) == 0 ? 49152 : (s & 3) == 1 ? 83968 : (s & 3) == 2 ? 118784 : 0; }
constexpr int gb_store(int st) { return st == 1 ? 2 : st == 2 ? 2 : (st >= 3 && st <= 6) ? 1 : 0; }
constexpr int gb_extra(int st) { return (st == 4 || st == 6) ? 1 : 0; }
constexpr int gb_wait(int st) {
  return st >= 7 ? 0 : (st > 0 ? gb_store(st - 1) + gb_extra(st - 1) : 0) + 6 + gb_store(st);
}

DI void h_signal(u32* p, int lane) {
  if (lane == 0) __hip_atomic_store(p, 1u, __ATOMIC_RELAXED, __HIP_MEMORY_SCOPE_AGENT);
}
DI void wait_flags(const u32* p) {
  u32 spins = 0;
  while (!__all(__hip_atomic_load(p, __ATOMIC_RELAXED, __HIP_MEMORY_SCOPE_AGENT) != 0u)) {
    __builtin_amdgcn_s_sleep(1);
    if (++spins > (1u << 24)) break;
  }
  __builtin_amdgcn_fence(__ATOMIC_ACQUIRE, "wavefront");
}
DI u32 h_dword(float g0, float g1, float u0, float u1) {
  const u32 g = pack_bf(g0, g1), u = pack_bf(u0, u1);
  return pack_bf(bf_lo(g) * bf_lo(u), bf_hi(g) * bf_hi(u));
}
DI void poll_issue(u32& v, const u32* p) {
  asm volatile("global_load_dword %0, %1, off sc1" : "=v"(v) : "v"(p) : "memory");
}
DI void poll_check(u32& v, u32* p, u32 target) {
  asm volatile("" : "+v"(v));
  if (__builtin_amdgcn_readfirstlane(v) < target) wait_count(p, target);
}

DI void flags_check(u32& v, const u32* p) {
  asm volatile("" : "+v"(v));
  if (!__all(v != 0u)) wait_flags(p);
}

struct G2Dma {
  srd_t sw, sx;
  u32 wsoffA, wsoffB, xsoff, m0w, m0x, woff, soff, xoff;
};
DI void g2_piece(const G2Dma& d, int lane, int st, int i) {
  const u32 bo = g_slot(st);
  const int kb = st < 6 ? 6 * st : st < 12 ? 6 * (st - 6) + 1 : 6 * (st - 12) + 4;
  const int kb2 = kb + (st < 12 ? 2 : 1);
  const u32 wo = (st < 12 ? d.wsoffA : d.wsoffB) + (u32)kb * (32 * D_ * 2);
  if (i == 0) dma16(d.m0w + bo, d.woff, d.sw, wo);
  else if (i == 1) dma16(d.m0w + bo + 1024, d.woff, d.sw, wo + 128);
  else if (i == 2) { if (lane < 16) dma16(d.m0w + bo + 2048, d.soff, d.sw, wo + 256); }
  else if (i == 3) dma16_sc1(d.m0x + bo, d.xoff, d.sx, d.xsoff + kb * 1024);
  else dma16_sc1(d.m0x + bo + 1024, d.xoff, d.sx, d.xsoff + kb2 * 1024);
}

DI void gemm1_body(u32 sb, srd_t sw, srd_t sx, u32 wsoff, u32 xsoff, int w, int lane, int wm, int wn,
                   char* ws, u32 hoffA, u32* hsig, const G2Dma& g2, const u32* hflg, v4u (&hB)[4]) {
  constexpr int NU = 8;
  const int g = lane >> 4, qq = (lane & 15) >> 2, p = lane & 3;
  const int rr = 4 * (g & 1) + qq;
  const int wl0 = rr * 128 + (2 * (rr >> 1)) * 16 + p * 8;
  const u32 woff = (u32)((lane >> 3) * (F_ * 2) + ((lane & 7) ^ (2 * (lane >> 4))) * 16);
  const u32 xoff = lane * 16;
  v8s wf[3], xf[2][4];
  const u32 m0wB = sb + (w & 3) * 2048 + (w >> 2) * 1024;
  const u32 m0xB = sb + 16384 + w * 4096;
  auto dmaB = [&](int st, int i) {
    const u32 bo = b_slot(st);
    if (i < 2) dma16(m0wB + bo + i * 8192, woff, sw, wsoff + (u32)st * (64 * F_ * 2) + 256 + i * (32 * F_ * 2));
    else dma16(m0xB + bo + (i - 2) * 1024, xoff, sx, xsoff + st * 2048 + ((i - 2) >> 1) * (18 * 1024) + ((i - 2) & 1) * 1024);
  };
  v4f accA[2][4][4];
  {
    u32 wp[4], xp;
#pragma unroll
    for (int t = 0; t < 4; ++t) wp[t] = opaque((sb + wl0 + (g >> 1) * 4096 + wn * 1024) ^ (t << 5));
    xp = opaque(sb + 16384 + wm * 4096 + lane * 16);
    const u32 m0w = sb + (w & 3) * 4096 + (w >> 2) * 2048;
    const u32 m0x = sb + 16384 + w * 2048;
    auto dma_piece = [&](int st, int i) {
      const u32 bo = (st & 3) * A_STAGE;
      if (i < 2) dma16(m0w + bo + i * 1024, woff, sw, wsoff + (u32)st * (32 * F_ * 2) + i * 128);
      else dma16(m0x + bo + (i - 2) * 1024, xoff, sx, xsoff + st * 1024 + (i - 2) * (18 * 1024));
    };
#pragma unroll
    for (int st = 0; st < 3; ++st)
#pragma unroll
      for (int i = 0; i < 4; ++i) dma_piece(st, i);
    wait_vm<8>();
    stage_barrier();
#pragma unroll
    for (int mb = 0; mb < 4; ++mb) xf[0][mb] = ld_frag(xp, mb * 1024);
    wf[0] = tr_frag(wp[0], 0, 8192);
    wf[1] = tr_frag(wp[0], 2048, 2048 + 8192);
    dma_piece(3, 0); dma_piece(3, 1);
    SCHED0();
    static_for<18 * NU>([&](auto UC) {
      constexpr int U = decltype(UC)::value, st = U / NU, u = U % NU;
      constexpr int T = u >> 1, gu = u & 1;
      constexpr bool trans = (u == NU - 2 && st + 1 < 18);
      constexpr bool rd = (u + 2 < NU) || (st + 1 < 18);
      constexpr int un = (u + 2) % NU;
      auto mm = [&](int mb) {
        if constexpr (st == 0) mfma16_first(accA[gu][T][mb], wf[U % 3], xf[st & 1][mb]);
        else mfma16(accA[gu][T][mb], wf[U % 3], xf[st & 1][mb]);
        SCHED0();
      };
      v4s lo, hi;
      mm(0);
      if constexpr (!trans && rd) { lo = tr_half(wp[un >> 1], (un & 1) * 2048); SCHED0(); }
      mm(1);
      if constexpr (trans) {
        if constexpr (st + 1 <= 15) wait_vm<8>(); else if constexpr (st + 1 == 16) wait_vm<4>(); else wait_vm<8>();
        stage_barrier();
        constexpr int dl = (((st + 1) & 3) == 0) ? -3 * A_STAGE : A_STAGE;
#pragma unroll
        for (int t = 0; t < 4; ++t) wp[t] = opaque(wp[t] + dl);
        xp = opaque(xp + dl);
        lo = tr_half(wp[un >> 1], (un & 1) * 2048);
      }
      if constexpr (rd) { hi = tr_half(wp[un >> 1], (un & 1) * 2048 + 8192); wf[(U + 2) % 3] = join(lo, hi); }
      SCHED0();
      mm(2);
      if constexpr (u >= NU - 2 && st + 1 < 18) {
        xf[(st + 1) & 1][2 * (u - 6)] = ld_frag(xp, (2 * (u - 6)) * 1024);
        xf[(st + 1) & 1][2 * (u - 6) + 1] = ld_frag(xp, (2 * (u - 6) + 1) * 1024);
      }
      SCHED0();
      mm(3);
      if constexpr (u >= NU - 2) { if constexpr (st + 4 < 18) dma_piece(st + 4, u - (NU - 2)); }
      else if constexpr (u < 2) { if constexpr (st + 3 < 18) dma_piece(st + 3, u + 2); }
      constexpr int qB = U - (15 * NU + 6);
      if constexpr (qB >= 0 && qB < 12) dmaB(qB / 6, qB % 6);
      SCHED0();
    });
  }
  {
    v4f acc[2][2][4];
    v4u hA[8];
    u32 wp[2], xp, pollA = 0;
#pragma unroll
    for (int t = 0; t < 2; ++t) wp[t] = opaque((sb + b_slot(0) + wl0 + (g >> 1) * 2048) ^ ((2 * wn + t) << 5));
    xp = opaque(sb + b_slot(0) + 16384 + wm * 8192 + lane * 16);
    auto storeA = [&](int i) {
      __builtin_amdgcn_raw_buffer_store_b128(hA[i], g2.sx, lane * 16, hoffA + (i & 3) * (144 * 1024) + (i >> 2) * 1024, 16);
    };
    auto conv1 = [&](int cq) {
      const int gi = cq >> 2, pr = gi >> 2, mb = gi & 3, cT = 2 * pr + ((cq >> 1) & 1), ch = cq & 1;
      hA[gi][cq & 3] = h_dword(accA[0][cT][mb][2 * ch], accA[0][cT][mb][2 * ch + 1], accA[1][cT][mb][2 * ch], accA[1][cT][mb][2 * ch + 1]);
    };
    wait_vm<6>();
    stage_barrier();
#pragma unroll
    for (int mb = 0; mb < 4; ++mb) xf[0][mb] = ld_frag(xp, mb * 2048);
    wf[0] = tr_frag(wp[0], 0, 4096);
    wf[1] = tr_frag(wp[0], 1024, 1024 + 4096);
    dmaB(2, 0); dmaB(2, 1);
    mfma_drain();
    SCHED0();
    static_for<9 * NU>([&](auto UC) {
      constexpr int U = decltype(UC)::value, st = U / NU, u = U % NU;
      constexpr int T = (u >> 1) & 1, gu = u & 1;
      constexpr bool trans = (u == NU - 2 && st + 1 < 9);
      constexpr bool rd = (u + 2 < NU) || (st + 1 < 9);
      constexpr int un = (u + 2) % NU;
      constexpr int wo = (un >> 2) * 8192 + (un & 1) * 1024;
      auto mm = [&](int mb) {
        if constexpr (st == 0 && u < 4) mfma16_first(acc[gu][T][mb], wf[U % 3], xf[u >> 2][mb]);
        else mfma16(acc[gu][T][mb], wf[U % 3], xf[u >> 2][mb]);
        SCHED0();
      };
      v4s lo, hi;
      mm(0);
      if constexpr (!trans && rd) { lo = tr_half(wp[(un >> 1) & 1], wo); }
      if constexpr (U < 16) conv1(2 * U);
      SCHED0();
      mm(1);
      if constexpr (trans) {
        wait_vm<gb_wait(st)>();
        stage_barrier();
        if constexpr (st == 4) h_signal(hsig, lane);
        if constexpr (st == 6) poll_issue(pollA, hflg);
        if constexpr (st == 7) { h_signal(hsig + 64 * 32, lane); flags_check(pollA, hflg); }
        constexpr int dl = b_slot(st + 1) - b_slot(st);
#pragma unroll
        for (int t = 0; t < 2; ++t) wp[t] = opaque(wp[t] + dl);
        xp = opaque(xp + dl);
        lo = tr_half(wp[(un >> 1) & 1], wo);
      }
      if constexpr (rd) { hi = tr_half(wp[(un >> 1) & 1], wo + 2 * 2048); wf[(U + 2) % 3] = join(lo, hi); }
      SCHED0();
      mm(2);
      if constexpr (u == 1 || u == 2) { xf[1][2 * (u - 1)] = ld_frag(xp, (2 * (u - 1)) * 2048 + 1024); xf[1][2 * (u - 1) + 1] = ld_frag(xp, (2 * (u - 1) + 1) * 2048 + 1024); }
      if constexpr (u >= NU - 2 && st + 1 < 9) { xf[0][2 * (u - 6)] = ld_frag(xp, (2 * (u - 6)) * 2048); xf[0][2 * (u - 6) + 1] = ld_frag(xp, (2 * (u - 6) + 1) * 2048); }
      if constexpr (U < 16) conv1(2 * U + 1);
      SCHED0();
      mm(3);
      if constexpr (u >= NU - 2) { if constexpr (st + 3 < 9) dmaB(st + 3, u - (NU - 2)); }
      else if constexpr (u < 4) { if constexpr (st + 2 < 9) dmaB(st + 2, u + 2); }
      if constexpr ((st == 1 || st == 2) && (u == 4 || u == 5)) storeA(2 * (st - 1) + u - 4);
      if constexpr (st >= 3 && st <= 6 && u == 4) storeA(st + 1);
      constexpr int qG = U - (7 * NU + 6);
      if constexpr (qG >= 0 && qG < 4) { g2_piece(g2, lane, (2 * qG) / 3, (2 * qG) % 3); g2_piece(g2, lane, (2 * qG + 1) / 3, (2 * qG + 1) % 3); }
      if constexpr (qG == 4) g2_piece(g2, lane, 2, 2);
      if constexpr (qG >= 5 && qG < 8) { g2_piece(g2, lane, qG - 5, 3); g2_piece(g2, lane, qG - 5, 4); }
      SCHED0();
    });
    mfma_drain();
    SCHED0();
#pragma unroll
    for (int mb = 0; mb < 4; ++mb)
#pragma unroll
      for (int c = 0; c < 4; ++c)
        hB[mb][c] = h_dword(acc[0][c >> 1][mb][2 * (c & 1)], acc[0][c >> 1][mb][2 * (c & 1) + 1],
                            acc[1][c >> 1][mb][2 * (c & 1)], acc[1][c >> 1][mb][2 * (c & 1) + 1]);
  }
}

template <int NT, bool PRE>
DI void gemm2_body(char* smem, u32 sb, const G2Dma& g2, int w, int lane, int kg, int wm, int tbase, int tile, int ksl,
                   const u16* __restrict__ xin, u16* __restrict__ out, char* ws, u32* hsigB, const v4u (&hB)[4],
                   u32 hBoff) {
  constexpr int NU = NT;
  constexpr int NO = (2 * NT + 3) / 4;
  v4f acc[NT][4];
  const int g = lane >> 4, qq = (lane & 15) >> 2, p = lane & 3;
  const int rr = 4 * (g & 1) + qq;
  const int wl0 = rr * 128 + (2 * (rr >> 1)) * 16 + p * 8;
  u32 wp[NT], xp;
#pragma unroll
  for (int t = 0; t < 4; ++t)
    wp[t] = opaque((sb + g_slot(0) + wl0 + ((g >> 1) + 4 * kg) * 2304 + (NT == 5 ? 1024 : 0)) ^ (t << 5));
  if constexpr (NT == 5) wp[4] = opaque(sb + g_slot(0) + 2048 + rr * 32 + p * 8 + ((g >> 1) + 4 * kg) * 2304);
  xp = opaque(sb + g_slot(0) + G_XOFF + wm * 8192 + kg * 1024 + lane * 16);
  auto rdW = [&](int u) -> v8s { return tr_frag(wp[u], 0, 2 * 2304); };
  const u32* hflg = (const u32*)(ws + WS_HCNT) + ((tile >> 2) * 4 + ksl) * 32 + (lane < 24 ? lane : 0);
  u32* ycnt = (u32*)(ws + WS_YCNT);
  v8s wf[3], xf[2][4];
  u32 pollB = 0, pollY = 0;
  const int j = 2 * wm + kg;
  const int tok0 = (tile >> 2) * 128 + j * 32 + (lane & 15) + 16 * (ksl & 1);
  const int dcol = (tile & 3) * 144 + tbase * 16 + (lane >> 4) * 4 + 16 * (ksl >> 1);
  v2u xv[NO]; u32 yv[NO][4]; v2f stt;
  constexpr int NL = 1 + 5 * NO;
  char* xw = smem + ((wm * 2 + kg) * 9 + tbase) * 2048 + lane * 16;
  if constexpr (!PRE) {
#pragma unroll
    for (int st = 0; st < 3; ++st)
#pragma unroll
      for (int i = 0; i < 3; ++i) g2_piece(g2, lane, st, i);
    wait_flags(hflg);
#pragma unroll
    for (int st = 0; st < 3; ++st) { g2_piece(g2, lane, st, 3); g2_piece(g2, lane, st, 4); }
  }
  wait_vm<4>();
  stage_barrier();
#pragma unroll
  for (int mb = 0; mb < 4; ++mb) xf[0][mb] = ld_frag(xp, mb * 2048);
  wf[0] = rdW(0);
  wf[1] = rdW(1);
  g2_piece(g2, lane, 3, 0); g2_piece(g2, lane, 3, 1);
  SCHED0();
  static_for<18 * NU>([&](auto UC) {
    constexpr int U = decltype(UC)::value, st = U / NU, u = U % NU;
    constexpr int SB = PRE ? 2 : 0;
    constexpr bool trans = (u == NU - 2 && st + 1 < 18);
    constexpr bool rd = (u + 2 < NU) || (st + 1 < 18);
    constexpr int un = (u + 2) % NU;
    auto mm = [&](int mb) {
      if constexpr (st == 0) mfma16_first(acc[u][mb], wf[U % 3], xf[st & 1][mb]);
      else mfma16(acc[u][mb], wf[U % 3], xf[st & 1][mb]);
      SCHED0();
    };
    v4s lo, hi;
    mm(0);
    if constexpr (!trans && rd) { lo = tr_half(wp[un], 0); SCHED0(); }
    mm(1);
    if constexpr (trans) {
      constexpr int NW = st == 0 ? 7 + SB : st == 1 ? 11 + 2 * SB : st == 2 ? 10 + SB : st == 3 ? 10 + SB
                         : st + 1 <= 15 ? 10 : st + 1 == 16 ? 5 : 0;
      wait_vm<NW>();
      stage_barrier();
      if constexpr (st == 0) poll_issue(pollB, hflg + 64 * 32);
      if constexpr (st == 2) flags_check(pollB, hflg + 64 * 32);
      if constexpr (PRE && st == 4) h_signal(hsigB, lane);
      if constexpr (st == 6) poll_issue(pollB, hflg + 128 * 32);
      if constexpr (st == 8) flags_check(pollB, hflg + 128 * 32);
      if constexpr (st == 12) poll_issue(pollY, ycnt);
      constexpr int dl = g_slot(st + 1) - g_slot(st);
#pragma unroll
      for (int t = 0; t < NT; ++t) wp[t] = opaque(wp[t] + dl);
      xp = opaque(xp + dl);
      lo = tr_half(wp[un], 0);
    }
    if constexpr (rd) { hi = tr_half(wp[un], 2 * 2304); wf[(U + 2) % 3] = join(lo, hi); }
    SCHED0();
    mm(2);
    if constexpr (u >= NU - 2 && st + 1 < 18) {
      xf[(st + 1) & 1][2 * (u - (NU - 2))] = ld_frag(xp, (2 * (u - (NU - 2))) * 2048);
      xf[(st + 1) & 1][2 * (u - (NU - 2)) + 1] = ld_frag(xp, (2 * (u - (NU - 2)) + 1) * 2048);
    }
    if constexpr (u == 0) { if constexpr (st + 3 < 18) g2_piece(g2, lane, st + 3, 2); }
    SCHED0();
    mm(3);
    if constexpr (u >= NU - 2) { if constexpr (st + 4 < 18) g2_piece(g2, lane, st + 4, u - (NU - 2)); }
    else if constexpr (u == 0) { if constexpr (st + 3 < 18) g2_piece(g2, lane, st + 3, 3); }
    else if constexpr (u == 1) { if constexpr (st + 3 < 18) g2_piece(g2, lane, st + 3, 4); }
    if constexpr (PRE && u == 1 && st < 2) {
#pragma unroll
      for (int mb = 2 * st; mb < 2 * st + 2; ++mb)
        __builtin_amdgcn_raw_buffer_store_b128(hB[mb], g2.sx, lane * 16, hBoff + mb * (144 * 1024), 16);
    }
    if constexpr (st == 17 && u >= 1) {
#pragma unroll
      for (int b2 = 0; b2 < 2; ++b2) *(v4f*)(xw + ((u - 1) * 2 + b2) * 1024) = kg ? acc[u - 1][b2] : acc[u - 1][2 + b2];
    }
    SCHED0();
  });
  mfma_drain();
  SCHED0();
#pragma unroll
  for (int b2 = 0; b2 < 2; ++b2) *(v4f*)(xw + ((NT - 1) * 2 + b2) * 1024) = kg ? acc[NT - 1][b2] : acc[NT - 1][2 + b2];
  stage_barrier();
  v4f r[2 * NT];
  {
    const char* rd = smem + ((wm * 2 + (kg ^ 1)) * 9 + tbase) * 2048 + lane * 16;
#pragma unroll
    for (int T = 0; T < NT; ++T)
#pragma unroll
      for (int b2 = 0; b2 < 2; ++b2) {
        const v4f o = *(const v4f*)(rd + (T * 2 + b2) * 1024);
        r[T * 2 + b2] = kg ? (o + acc[T][2 + b2]) : (acc[T][b2] + o);
      }
  }
  const srd_t sp = make_srd(ws + WS_PART, 64 * 4 * 8 * 4 * 3072);
#pragma unroll
  for (int f = 0; f < 2 * NT; ++f)
    if ((f & 3) != ksl)
      __builtin_amdgcn_raw_buffer_store_b128(__builtin_bit_cast(v4u, r[f]), sp, lane * 16,
                                             (u32)((((tile * 4 + (f & 3)) * 8 + w) * 4 + ksl) * 3072 + (f >> 2) * 1024), 16);
  v4f own[NO];
#pragma unroll
  for (int i = 0; i < NO; ++i) {
    const v4f a0 = r[4 * i], a1 = r[4 * i + 1];
    const v4f a2 = (4 * i + 2 < 2 * NT) ? r[(4 * i + 2 < 2 * NT) ? 4 * i + 2 : 0] : a0;
    const v4f a3 = (4 * i + 3 < 2 * NT) ? r[(4 * i + 3 < 2 * NT) ? 4 * i + 3 : 0] : a0;
    own[i] = ksl == 0 ? a0 : ksl == 1 ? a1 : ksl == 2 ? a2 : a3;
  }
  SCHED0();
  poll_check(pollY, ycnt, 576u);
  {
    const srd_t sy = make_srd(ws + WS_YT, S_ * D_ * 2);
    stt = ((const v2f*)(ws + WS_STATS))[tok0];
#pragma unroll
    for (int i = 0; i < NO; ++i) {
      const int dc = (4 * i + ksl < 2 * NT) ? dcol + 32 * i : dcol;
      xv[i] = *(const v2u*)(xin + (size_t)tok0 * D_ + dc);
#pragma unroll
      for (int r = 0; r < 4; ++r) yv[i][r] = __builtin_amdgcn_raw_buffer_load_b16(sy, (u32)(((dc + r) * S_ + tok0) * 2), 0, 16);
    }
  }
  SCHED0();
  wait_vm<NL>();
  u32* flags = (u32*)(ws + WS_CNT);
  if (lane < 4 && lane != ksl)
    __hip_atomic_store(flags + ((tile * 4 + lane) * 8 + w) * 16 + ksl, 1u, __ATOMIC_RELAXED, __HIP_MEMORY_SCOPE_AGENT);
  float xa[NO][4];
#pragma unroll
  for (int i = 0; i < NO; ++i)
#pragma unroll
    for (int e = 0; e < 4; ++e) {
      const float xf_ = (e & 1) ? bf_hi(xv[i][e >> 1]) : bf_lo(xv[i][e >> 1]);
      const float xn = rbf(rbf(xf_ - stt[0]) * stt[1]);
      xa[i][e] = rbf(xf_ + rbf(xn * bf_lo(yv[i][e])));
    }
  {
    u32* fl = flags + ((tile * 4 + ksl) * 8 + w) * 16;
    u32 spins = 0;
    for (;;) {
      const u32 v = (lane < 4 && lane != ksl) ? __hip_atomic_load(fl + lane, __ATOMIC_RELAXED, __HIP_MEMORY_SCOPE_AGENT) : 1u;
      if (__all(v != 0u)) break;
      __builtin_amdgcn_s_sleep(1);
      if (++spins > (1u << 24)) break;
    }
  }
  __builtin_amdgcn_fence(__ATOMIC_ACQUIRE, "wavefront");
  v4f pp[3][NO];
#pragma unroll
  for (int i3 = 0; i3 < 3; ++i3) {
    const int src = i3 + (i3 >= ksl ? 1 : 0);
    const u32 po = (u32)((((tile * 4 + ksl) * 8 + w) * 4 + src) * 3072);
#pragma unroll
    for (int i = 0; i < NO; ++i)
      pp[i3][i] = __builtin_bit_cast(v4f, __builtin_amdgcn_raw_buffer_load_b128(sp, lane * 16, po + i * 1024, 16));
  }
#pragma unroll
  for (int i = 0; i < NO; ++i) {
    const v4f s0 = ksl == 0 ? own[i] : pp[0][i];
    const v4f s1 = ksl == 1 ? own[i] : (ksl < 1 ? pp[0][i] : pp[1][i]);
    const v4f s2 = ksl == 2 ? own[i] : (ksl < 2 ? pp[1][i] : pp[2][i]);
    const v4f s3 = ksl == 3 ? own[i] : pp[2][i];
    const v4f B = (s0 + s1) + (s2 + s3);
    const float o0 = xa[i][0] + rbf(B[0]), o1 = xa[i][1] + rbf(B[1]);
    const float o2 = xa[i][2] + rbf(B[2]), o3 = xa[i][3] + rbf(B[3]);
    if (4 * i + ksl < 2 * NT)
      *(v2u*)(out + (size_t)tok0 * D_ + dcol + 32 * i) = v2u{pack_bf(o0, o1), pack_bf(o2, o3)};
  }
}

__global__ __launch_bounds__(512, 2) void fused_kernel(const u16* __restrict__ Wg, const u16* __restrict__ Wu,
                                                       const u16* __restrict__ Wd, const u16* __restrict__ ssmk,
                                                       const u16* __restrict__ ssmD, const u16* __restrict__ xin,
                                                       u16* __restrict__ out, char* __restrict__ ws) {
  extern __shared__ __attribute__((aligned(1024))) char smem[];
  unsigned long long pc0;
  asm volatile("s_getpc_b64 %0" : "=s"(pc0));
  const int bid = blockIdx.x;
  const int xcd = bid & 7, idx = bid >> 3;
  const int tid = threadIdx.x, lane = tid & 63;
  const int w = __builtin_amdgcn_readfirstlane(tid >> 6);
  const int ksl = xcd >> 1;
  const bool isconv = idx >= 24;
  const int tile2 = isconv ? (2 * (idx - 24) + (xcd & 1)) * 4 + 3 : (2 * (idx / 3) + (xcd & 1)) * 4 + idx % 3;
  const u32 sb = lds_addr(smem);
  G2Dma g2;
  g2.sw = make_srd(Wd, D_ * F_ * 2);
  g2.sx = make_srd(ws + WS_HT, S_ * F_ * 2);
  g2.wsoffA = (u32)((ksl * 1152 + 8 * (w & 3) + (w >> 2) * 64) * (D_ * 2) + (tile2 & 3) * 288);
  g2.wsoffB = (u32)((ksl * 1152 + 8 * w) * (D_ * 2) + (tile2 & 3) * 288);
  g2.xsoff = (u32)((((tile2 >> 2) * 8 + w) * 144 + ksl * 36) * 1024);
  g2.m0w = sb + w * 2304;
  g2.m0x = sb + G_XOFF + w * 2048;
  g2.woff = (u32)((lane >> 3) * (D_ * 2) + ((lane & 7) ^ (2 * (lane >> 4))) * 16);
  g2.soff = (u32)(((lane >> 1) & 7) * (D_ * 2) + (lane & 1) * 16);
  g2.xoff = lane * 16;
  const int kg2 = w >> 2, wm2 = (w >> 1) & 1, wn2 = (w & 1) ^ kg2;
  v4u hB[4];
  if (isconv) {
    const int cb = xcd * 8 + (idx - 24);
    if (w == 7) {
      const int c8 = idx - 24;
      u32 t[44];
#pragma unroll
      for (int i = 0; i < 2; ++i) {
        int e = (i * 8 + c8) * 64 + lane;
        e = e < 960 ? e : 0;
        t[42 + i] = *(const u32*)((pc0 & ~127ull) + (size_t)e * 128);
      }
#pragma unroll
      for (int i = 0; i < 14; ++i) {
        int e = (i * 8 + c8) * 64 + lane;
        e = e < 6912 ? e : 0;
        const int row = e / 12, r12 = e % 12, mtx = r12 / 6, l6 = r12 % 6;
        t[i] = *(const u32*)((const char*)(mtx ? Wu : Wg) + (size_t)row * (F_ * 2) + xcd * 1152 + (l6 >> 1) * 384 + (l6 & 1) * 128);
      }
#pragma unroll
      for (int i = 0; i < 7; ++i) {
        int e = (i * 8 + c8) * 64 + lane;
        e = e < 3456 ? e : 0;
        const int row = e / 6, r6 = e % 6, mtx = r6 / 3, l3 = r6 % 3;
        t[14 + i] = *(const u32*)((const char*)(mtx ? Wu : Wg) + (size_t)row * (F_ * 2) + xcd * 1152 + l3 * 384 + 256);
      }
#pragma unroll
      for (int i = 0; i < 21; ++i) {
        int e = (i * 8 + c8) * 64 + lane;
        e = e < 10368 ? e : 0;
        t[21 + i] = *(const u32*)((const char*)Wd + (size_t)ksl * (1152 * 1152) + (size_t)e * 128);
      }
      u32 a = 0;
#pragma unroll
      for (int i = 0; i < 44; ++i) a ^= t[i];
      asm volatile("" :: "v"(a));
    }
#pragma unroll 1
    for (int c = w; c < 9 && w < 7; c += 7) {
      conv_channel(smem + w * C_WAVE, cb * 9 + c, lane, ssmk, ssmD, ws);
    }
    stage_barrier();
#pragma unroll
    for (int i = 0; i < 4; ++i) hB[i] = v4u{0u, 0u, 0u, 0u};
    if (wn2 == 0) gemm2_body<5, false>(smem, sb, g2, w, lane, kg2, wm2, 4, tile2, ksl, xin, out, ws, nullptr, hB, 0u);
    else          gemm2_body<4, false>(smem, sb, g2, w, lane, kg2, wm2, 0, tile2, ksl, xin, out, ws, nullptr, hB, 0u);
  } else {
    const int tile_n = xcd * 3 + idx % 3, tile_m = idx / 3;
    const int wm = w & 3, wn = w >> 2;
    const srd_t sw = make_srd((w >> 2) ? Wu : Wg, D_ * F_ * 2);
    const srd_t sx = make_srd(ws + WS_XNT, S_ * D_ * 2);
    const u32 wsoff = (u32)((8 * (w & 3)) * (F_ * 2) + tile_n * 384);
    const u32 xsoff = (u32)((tile_m * 16 + 2 * w) * (18 * 1024));
    u32* hsigA = (u32*)(ws + WS_HCNT) + ((2 * tile_m + (wm >> 1)) * 4 + ksl) * 32 + (tile_n % 6) * 4 + (wm & 1) * 2 + wn;
    const u32* hflg = (const u32*)(ws + WS_HCNT) + ((tile2 >> 2) * 4 + ksl) * 32 + (lane < 24 ? lane : 0);
    const u32 hoff = (u32)(((tile_m * 16 + wm * 4) * 144 + tile_n * 6) * 1024);
    gemm1_body(sb, sw, sx, wsoff, xsoff, w, lane, wm, wn, ws, hoff + 2 * wn * 1024, hsigA, g2, hflg, hB);
    const u32 hBoff = hoff + (4 + wn) * 1024;
    if (wn2 == 0) gemm2_body<5, true>(smem, sb, g2, w, lane, kg2, wm2, 4, tile2, ksl, xin, out, ws, hsigA + 128 * 32, hB, hBoff);
    else          gemm2_body<4, true>(smem, sb, g2, w, lane, kg2, wm2, 0, tile2, ksl, xin, out, ws, hsigA + 128 * 32, hB, hBoff);
  }
}

extern "C" void kernel_launch(void* const* d_in, const int* in_sizes, int n_in, void* d_out, int out_size,
                              void* d_ws, size_t ws_size, hipStream_t stream) {
  const u16* q = (const u16*)d_in[0];
  const u16* k = (const u16*)d_in[1];
  const u16* v = (const u16*)d_in[2];
  const u16* shk = (const u16*)d_in[3];
  const u16* ssmk = (const u16*)d_in[4];
  const u16* ssmD = (const u16*)d_in[5];
  const u16* Wg = (const u16*)d_in[7];
  const u16* Wu = (const u16*)d_in[8];
  const u16* Wd = (const u16*)d_in[9];
  char* ws = (char*)d_ws;
  (void)hipFuncSetAttribute((const void*)fused_kernel, hipFuncAttributeMaxDynamicSharedMemorySize, H_LDS);
  pre_kernel<<<256 + 288, 256, 0, stream>>>(q, k, v, shk, ws);
  fused_kernel<<<256, 512, H_LDS, stream>>>(Wg, Wu, Wd, ssmk, ssmD, q, (u16*)d_out, ws);
}
```

```cpp
#include <hip/hip_runtime.h>
#include <hip/hip_bf16.h>
#include <stdint.h>
#include <utility>

typedef unsigned short u16;
typedef unsigned int u32;
typedef short v4s __attribute__((ext_vector_type(4)));
typedef short v8s __attribute__((ext_vector_type(8)));
typedef float v4f __attribute__((ext_vector_type(4)));
typedef float v2f __attribute__((ext_vector_type(2)));
typedef u32 v4u __attribute__((ext_vector_type(4)));
typedef u32 v2u __attribute__((ext_vector_type(2)));
typedef __bf16 v2bf __attribute__((ext_vector_type(2)));
typedef __attribute__((address_space(3))) v4s* lds_v4s_ptr;

constexpr int S_ = 2048, D_ = 576, F_ = 4608;

#define DI __device__ __forceinline__

template <int... I, class Fn>
DI void static_for_impl(std::integer_sequence<int, I...>, Fn&& f) { (f(std::integral_constant<int, I>{}), ...); }
template <int N, class Fn>
DI void static_for(Fn&& f) { static_for_impl(std::make_integer_sequence<int, N>{}, f); }

DI float bf_lo(u32 w) { return __uint_as_float(w << 16); }
DI float bf_hi(u32 w) { return __uint_as_float(w & 0xffff0000u); }
DI float rbf(float f) { return (float)(__bf16)f; }
DI u32 pack_bf(float lo, float hi) {
  v2f t = {lo, hi};
  v2bf b = __builtin_convertvector(t, v2bf);
  return __builtin_bit_cast(u32, b);
}

template <int CTRL> DI float dpp_f(float v) {
  return __builtin_bit_cast(float, __builtin_amdgcn_update_dpp(0, __builtin_bit_cast(int, v), CTRL, 0xf, 0xf, false));
}
DI float sum32(float v) {
  v += dpp_f<0x128>(v);
  v += dpp_f<0x124>(v);
  v += dpp_f<0x122>(v);
  v += dpp_f<0x121>(v);
  return v + __shfl_xor(v, 16);
}
constexpr size_t WS_XNT = 0;
constexpr size_t WS_STATS = WS_XNT + (size_t)S_ * D_ * 2;
constexpr size_t WS_KVT = WS_STATS + (size_t)S_ * 8;
constexpr size_t WS_YT = WS_KVT + (size_t)S_ * D_ * 2;
constexpr size_t WS_HT = WS_YT + (size_t)S_ * D_ * 2;
constexpr size_t WS_PART = WS_HT + (size_t)S_ * F_ * 2;
constexpr size_t WS_CNT = WS_PART + (size_t)64 * 4 * 8 * 4 * 3072;
constexpr size_t WS_HCNT = WS_CNT + 2048 * 64;
constexpr size_t WS_YCNT = WS_HCNT + 3 * 64 * 128;

__global__ __launch_bounds__(256) void pre_kernel(const u16* __restrict__ x, const u16* __restrict__ kin,
                                                  const u16* __restrict__ vin, const u16* __restrict__ shk,
                                                  char* __restrict__ ws) {
  const int lane = threadIdx.x & 63, w = threadIdx.x >> 6;
  if (blockIdx.x < 8) *(v4u*)(ws + WS_CNT + (size_t)(blockIdx.x * 256 + threadIdx.x) * 64) = v4u{0u, 0u, 0u, 0u};
  else if (blockIdx.x < 15) *(v4u*)(ws + WS_HCNT + (size_t)((blockIdx.x - 8) * 256 + threadIdx.x) * 16) = v4u{0u, 0u, 0u, 0u};
  if (blockIdx.x < 256) {
    const int c = lane & 31;
    const int row = blockIdx.x * 8 + w * 2 + (lane >> 5);
    const v4u* src = (const v4u*)(x + (size_t)row * D_);
    v4u d[3];
    d[0] = src[c]; d[1] = src[c + 32]; d[2] = src[64 + (c & 7)];
    const float lastw = (c < 8) ? 1.f : 0.f;
    float f[3][8];
#pragma unroll
    for (int i = 0; i < 3; ++i)
#pragma unroll
      for (int j = 0; j < 4; ++j) { f[i][2 * j] = bf_lo(d[i][j]); f[i][2 * j + 1] = bf_hi(d[i][j]); }
    float s0 = 0.f, s1 = 0.f, s2 = 0.f;
#pragma unroll
    for (int j = 0; j < 8; ++j) { s0 += f[0][j]; s1 += f[1][j]; s2 += f[2][j]; }
    const float mean = sum32(s0 + s1 + s2 * lastw) * (1.0f / D_);
    float q0 = 0.f, q1 = 0.f, q2 = 0.f;
#pragma unroll
    for (int j = 0; j < 8; ++j) {
      const float a = f[0][j] - mean, b = f[1][j] - mean, e = f[2][j] - mean;
      q0 += a * a; q1 += b * b; q2 += e * e;
    }
    const float var = sum32(q0 + q1 + q2 * lastw) * (1.0f / D_);
    const float mb = rbf(mean);
    const float sd = rbf(sqrtf(rbf(rbf(var) + 1e-5f)));
    const float rinv = 1.0f / sd;
    if (c == 0) ((v2f*)(ws + WS_STATS))[row] = v2f{mb, rinv};
    char* xnt = ws + WS_XNT;
    const int tb = row >> 4, m = row & 15;
    const int cc = c & 3, T = cc >> 1, q0i = 2 * (cc & 1);
#pragma unroll
    for (int i = 0; i < 3; ++i) {
      u32 o[4];
#pragma unroll
      for (int j = 0; j < 4; ++j) {
        const u32 dd = pack_bf(f[i][2 * j] - mb, f[i][2 * j + 1] - mb);
        o[j] = pack_bf(bf_lo(dd) * rinv, bf_hi(dd) * rinv);
      }
      const int kb = 8 * i + (c >> 2);
      char* base = xnt + ((size_t)(tb * 18 + kb) * 64 + (q0i * 16 + m)) * 16 + 8 * T;
      if (i < 2 || c < 8) {
        *(v2u*)(base) = v2u{o[0], o[1]};
        *(v2u*)(base + 256) = v2u{o[2], o[3]};
      }
    }
  } else {
    const int b = blockIdx.x - 256;
    const int tt = b / 9, ct = b % 9;
    const int tg = lane >> 5, cx = lane & 31;
    const int s0 = tt * 64 + w * 16 + tg * 8, d0 = ct * 64 + cx * 2;
    u32 kk[11], vv[8];
#pragma unroll
    for (int i = 0; i < 11; ++i) {
      const int s = s0 - 3 + i;
      const u32 t = *(const u32*)(kin + (size_t)(s < 0 ? 0 : s) * D_ + d0);
      kk[i] = (i < 3 && s < 0) ? 0u : t;
    }
#pragma unroll
    for (int i = 0; i < 8; ++i) vv[i] = *(const u32*)(vin + (size_t)(s0 + i) * D_ + d0);
    const v4u w0 = *(const v4u*)(shk + (size_t)d0 * 4);
    u16* kvt = (u16*)(ws + WS_KVT);
#pragma unroll
    for (int ch = 0; ch < 2; ++ch) {
      const u32 wa = w0[2 * ch], wb = w0[2 * ch + 1];
      const float t0 = bf_lo(wa), t1 = bf_hi(wa), t2 = bf_lo(wb), t3 = bf_hi(wb);
      float kf[11];
#pragma unroll
      for (int i = 0; i < 11; ++i) kf[i] = ch ? bf_hi(kk[i]) : bf_lo(kk[i]);
      float r[8];
#pragma unroll
      for (int i = 0; i < 8; i += 2) {
        u32 a = pack_bf(kf[i] * t3, kf[i + 1] * t3);
        u32 p = pack_bf(kf[i + 1] * t2, kf[i + 2] * t2);
        a = pack_bf(bf_lo(a) + bf_lo(p), bf_hi(a) + bf_hi(p));
        p = pack_bf(kf[i + 2] * t1, kf[i + 3] * t1);
        a = pack_bf(bf_lo(a) + bf_lo(p), bf_hi(a) + bf_hi(p));
        p = pack_bf(kf[i + 3] * t0, kf[i + 4] * t0);
        a = pack_bf(bf_lo(a) + bf_lo(p), bf_hi(a) + bf_hi(p));
        r[i] = bf_lo(a) * (ch ? bf_hi(vv[i]) : bf_lo(vv[i]));
        r[i + 1] = bf_hi(a) * (ch ? bf_hi(vv[i + 1]) : bf_lo(vv[i + 1]));
      }
      v4u o = {pack_bf(r[7], r[6]), pack_bf(r[5], r[4]), pack_bf(r[3], r[2]), pack_bf(r[1], r[0])};
      *(v4u*)(kvt + (size_t)(d0 + ch) * S_ + s0) = o;
    }
  }
}

constexpr int G_XOFF = 18432;

typedef __attribute__((address_space(3))) v8s* lds_v8s_ptr;
typedef __attribute__((address_space(3))) char* lds_ptr;
DI v8s tr_frag(u32 p, int off_lo, int off_hi) {
  v4s a = __builtin_amdgcn_ds_read_tr16_b64_v4i16((lds_v4s_ptr)((lds_ptr)p + off_lo));
  v4s b = __builtin_amdgcn_ds_read_tr16_b64_v4i16((lds_v4s_ptr)((lds_ptr)p + off_hi));
  return __builtin_shufflevector(a, b, 0, 1, 2, 3, 4, 5, 6, 7);
}
DI v4s tr_half(u32 p, int off) { return __builtin_amdgcn_ds_read_tr16_b64_v4i16((lds_v4s_ptr)((lds_ptr)p + off)); }
DI v8s join(const v4s& a, const v4s& b) { return __builtin_shufflevector(a, b, 0, 1, 2, 3, 4, 5, 6, 7); }
DI v8s ld_frag(u32 p, int off) { return *(lds_v8s_ptr)((lds_ptr)p + off); }
DI u32 opaque(u32 v) { asm volatile("" : "+v"(v)); return v; }
DI u32 lds_addr(const void* p) { return (u32)(size_t)(lds_ptr)p; }
typedef __amdgpu_buffer_rsrc_t srd_t;
DI srd_t make_srd(const void* p, u32 bytes) {
  return __builtin_amdgcn_make_buffer_rsrc((void*)p, 0, bytes, 0x00020000);
}
DI void dma16(u32 m0v, u32 voff, srd_t srd, u32 soff) {
  asm volatile("s_mov_b32 m0, %0\n\ts_nop 0\n\tbuffer_load_dwordx4 %1, %2, %3 offen lds"
               :: "s"(m0v), "v"(voff), "s"(srd), "s"(soff) : "memory");
}
DI void dma16_sc1(u32 m0v, u32 voff, srd_t srd, u32 soff) {
  asm volatile("s_mov_b32 m0, %0\n\ts_nop 0\n\tbuffer_load_dwordx4 %1, %2, %3 offen sc1 lds"
               :: "s"(m0v), "v"(voff), "s"(srd), "s"(soff) : "memory");
}
DI void wait_count(u32* p, u32 target) {
  u32 spins = 0;
  while (__hip_atomic_load(p, __ATOMIC_RELAXED, __HIP_MEMORY_SCOPE_AGENT) < target) {
    __builtin_amdgcn_s_sleep(16);
    if (++spins > (1u << 22)) break;
  }
  __builtin_amdgcn_fence(__ATOMIC_ACQUIRE, "wavefront");
}
#define SCHED0() __builtin_amdgcn_sched_barrier(0)
DI void mfma16(v4f& c, const v8s& a, const v8s& b) {
  asm volatile("v_mfma_f32_16x16x32_bf16 %0, %1, %2, %0" : "+v"(c) : "v"(a), "v"(b));
}
DI void mfma16_first(v4f& c, const v8s& a, const v8s& b) {
  asm volatile("v_mfma_f32_16x16x32_bf16 %0, %1, %2, 0" : "=&v"(c) : "v"(a), "v"(b));
}
DI void mfma_drain() { asm volatile("s_nop 15\n\ts_nop 15" ::: "memory"); }
template <int N> DI void wait_vm() { asm volatile("s_waitcnt vmcnt(%0)" :: "n"(N) : "memory"); }
DI void stage_barrier() { asm volatile("s_waitcnt lgkmcnt(0)\n\ts_barrier" ::: "memory"); }

constexpr int C_KBYTES = 2080 * 8, C_XBYTES = 8 * 768, C_WAVE = C_KBYTES + C_XBYTES;

DI void conv_channel(char* kl, int ch, int lane, const u16* __restrict__ ssmk, const u16* __restrict__ ssmD,
                     char* __restrict__ ws) {
  char* xl = kl + C_KBYTES;
  const srd_t sk = make_srd(ssmk + (size_t)ch * S_, S_ * 2);
  const v4u* xs = (const v4u*)(ws + WS_KVT + (size_t)ch * S_ * 2);
  v4u xd[4];
#pragma unroll
  for (int it = 0; it < 4; ++it) xd[it] = xs[it * 64 + lane];
  u32 d0[17], d1[17], d2[17];
#pragma unroll
  for (int it = 0; it < 17; ++it) {
    const int dw = it * 64 + lane - 16;
    if (it == 0 || it == 16) {
      d0[it] = __builtin_amdgcn_raw_buffer_load_b32(sk, (u32)(dw * 4), 0, 0);
      asm volatile("" ::: "memory");
      d1[it] = __builtin_amdgcn_raw_buffer_load_b32(sk, (u32)(dw * 4 + 4), 0, 0);
      asm volatile("" ::: "memory");
      d2[it] = __builtin_amdgcn_raw_buffer_load_b32(sk, (u32)(dw * 4 + 8), 0, 0);
    } else {
      typedef u32 v3u __attribute__((ext_vector_type(3)));
      const v3u t = __builtin_amdgcn_raw_buffer_load_b96(sk, (u32)(dw * 4), 0, 0);
      d0[it] = t[0]; d1[it] = t[1]; d2[it] = t[2];
    }
  }
#pragma unroll
  for (int it = 0; it < 2; ++it) {
    const int z = it * 64 + lane;
    *(v4u*)(xl + (z >> 4) * 768 + (z & 15) * 16) = v4u{0u, 0u, 0u, 0u};
  }
#pragma unroll
  for (int it = 0; it < 4; ++it) {
    const int cidx = it * 64 + lane;
    *(v4u*)(xl + (cidx & 7) * 768 + ((cidx >> 3) + 16) * 16) = xd[it];
  }
#pragma unroll
  for (int it = 0; it < 17; ++it) {
    const int L = it * 64 + lane;
    v4u o = {d0[it], d1[it], __builtin_amdgcn_alignbit(d1[it], d0[it], 16), __builtin_amdgcn_alignbit(d2[it], d1[it], 16)};
    if (it < 16 || lane < 16) *(v4u*)(kl + L * 16) = o;
  }
  __builtin_amdgcn_s_waitcnt(0xC07F);
  __builtin_amdgcn_wave_barrier();

  const int i16 = lane & 15, q = lane >> 4;
  const u32 ap = opaque(lds_addr(kl) + (i16 - 8 * q - 7 + 32) * 8);
  const u32 bp = opaque(lds_addr(xl) + q * 768 + i16 * 16);
  v4f acc[4][2];
  v8s af[3][4], bfr[2][4];
  auto rdA = [&](int c, int j) -> v8s {
    const int delta = 64 * c + 16 * j;
    v4s lo = *(volatile __attribute__((address_space(3))) v4s*)((lds_ptr)ap + 8 * delta);
    v4s hi = *(volatile __attribute__((address_space(3))) v4s*)((lds_ptr)ap + 8 * delta + 32);
    return __builtin_shufflevector(lo, hi, 0, 1, 2, 3, 4, 5, 6, 7);
  };
  auto rdB = [&](int c, int n, int kb) -> v8s { return ld_frag(bp, 4 * kb * 768 + 16 * (16 * n - c + 16)); };
#pragma unroll
  for (int j = 0; j < 4; ++j) af[0][j] = rdA(0, j);
#pragma unroll
  for (int j = 0; j < 4; ++j) bfr[0][j] = rdB(0, j >> 1, j & 1);
  SCHED0();
  static_for<32>([&](auto CC) {
    constexpr int c = decltype(CC)::value;
    if constexpr (c + 1 < 32) {
#pragma unroll
      for (int j = 0; j < 4; ++j) af[(c + 1) % 3][j] = rdA(c + 1, j);
#pragma unroll
      for (int j = 0; j < 4; ++j)
        if (j >= 2 || c + 1 < 16) bfr[(c + 1) & 1][j] = rdB(c + 1, j >> 1, j & 1);
    }
    SCHED0();
#pragma unroll
    for (int n = 1; n >= 0; --n) {
      if (n == 0 && c >= 16) continue;
#pragma unroll
      for (int kb = 0; kb < 2; ++kb) {
#pragma unroll
        for (int ib = 0; ib < 4; ++ib) {
          const int j = ib - 2 * kb;
          if (c == 0 && j < 0) continue;
          const v8s& A = (j >= 0) ? af[c % 3][j >= 0 ? j : 0] : af[(c + 2) % 3][j < 0 ? j + 4 : 0];
          if (c == 0 && kb == 0) mfma16_first(acc[ib][n], A, bfr[c & 1][2 * n + kb]);
          else mfma16(acc[ib][n], A, bfr[c & 1][2 * n + kb]);
        }
      }
    }
    SCHED0();
  });
  mfma_drain();
  SCHED0();
  const float Dd = bf_lo((u32)ssmD[ch]);
  const srd_t sy = make_srd(ws + WS_YT + (size_t)ch * S_ * 2, S_ * 2);
#pragma unroll
  for (int n = 0; n < 2; ++n)
#pragma unroll
    for (int ib = 0; ib < 4; ++ib) {
      const int beta = 16 * n + i16;
      const v2u kvp = *(const v2u*)(xl + (2 * ib + (q >> 1)) * 768 + (beta + 16) * 16 + 8 * (1 - (q & 1)));
      const float k3 = bf_lo(kvp[0]), k2 = bf_hi(kvp[0]), k1 = bf_lo(kvp[1]), k0 = bf_hi(kvp[1]);
      const float y0 = rbf(acc[ib][n][0]) + rbf(k0 * Dd);
      const float y1 = rbf(acc[ib][n][1]) + rbf(k1 * Dd);
      const float y2 = rbf(acc[ib][n][2]) + rbf(k2 * Dd);
      const float y3 = rbf(acc[ib][n][3]) + rbf(k3 * Dd);
      __builtin_amdgcn_raw_buffer_store_b64(v2u{pack_bf(y0, y1), pack_bf(y2, y3)}, sy, (u32)((64 * beta + 16 * ib + 4 * q) * 2), 0, 16);
    }
  asm volatile("s_waitcnt vmcnt(0) lgkmcnt(0)" ::: "memory");
  if (lane == 0) __hip_atomic_fetch_add((u32*)(ws + WS_YCNT), 1u, __ATOMIC_RELAXED, __HIP_MEMORY_SCOPE_AGENT);
  __builtin_amdgcn_wave_barrier();
}

constexpr int H_LDS = 163840;
constexpr int A_STAGE = 32768;
constexpr int b_slot(int s) { return (s % 3) == 0 ? 65536 : (s % 3) == 1 ? 114688 : 0; }
constexpr int g_slot(int s) { return (s & 3) == 0 ? 49152 : (s & 3) == 1 ? 83968 : (s & 3) == 2 ? 118784 : 0; }
constexpr int gb_store(int st) { return st == 1 ? 2 : st == 2 ? 2 : (st >= 3 && st <= 6) ? 1 : 0; }
constexpr int gb_extra(int st) { return (st == 4 || st == 6) ? 1 : 0; }
constexpr int gb_wait(int st) {
  return st >= 7 ? 0 : (st > 0 ? gb_store(st - 1) + gb_extra(st - 1) : 0) + 6 + gb_store(st);
}

DI void h_signal(u32* p, int lane) {
  if (lane == 0) __hip_atomic_store(p, 1u, __ATOMIC_RELAXED, __HIP_MEMORY_SCOPE_AGENT);
}
DI void wait_flags(const u32* p) {
  u32 spins = 0;
  while (!__all(__hip_atomic_load(p, __ATOMIC_RELAXED, __HIP_MEMORY_SCOPE_AGENT) != 0u)) {
    __builtin_amdgcn_s_sleep(1);
    if (++spins > (1u << 24)) break;
  }
  __builtin_amdgcn_fence(__ATOMIC_ACQUIRE, "wavefront");
}
DI u32 h_dword(float g0, float g1, float u0, float u1) {
  const u32 g = pack_bf(g0, g1), u = pack_bf(u0, u1);
  return pack_bf(bf_lo(g) * bf_lo(u), bf_hi(g) * bf_hi(u));
}
DI void poll_issue(u32& v, const u32* p) {
  asm volatile("global_load_dword %0, %1, off sc1" : "=v"(v) : "v"(p) : "memory");
}
DI void poll_check(u32& v, u32* p, u32 target) {
  asm volatile("" : "+v"(v));
  if (__builtin_amdgcn_readfirstlane(v) < target) wait_count(p, target);
}

DI void flags_check(u32& v, const u32* p) {
  asm volatile("" : "+v"(v));
  if (!__all(v != 0u)) wait_flags(p);
}

struct G2Dma {
  srd_t sw, sx;
  u32 wsoffA, wsoffB, xsoff, m0w, m0x, woff, soff, xoff;
};
DI void g2_piece(const G2Dma& d, int lane, int st, int i) {
  const u32 bo = g_slot(st);
  const int kb = st < 6 ? 6 * st : st < 12 ? 6 * (st - 6) + 1 : 6 * (st - 12) + 4;
  const int kb2 = kb + (st < 12 ? 2 : 1);
  const u32 wo = (st < 12 ? d.wsoffA : d.wsoffB) + (u32)kb * (32 * D_ * 2);
  if (i == 0) dma16(d.m0w + bo, d.woff, d.sw, wo);
  else if (i == 1) dma16(d.m0w + bo + 1024, d.woff, d.sw, wo + 128);
  else if (i == 2) { if (lane < 16) dma16(d.m0w + bo + 2048, d.soff, d.sw, wo + 256); }
  else if (i == 3) dma16_sc1(d.m0x + bo, d.xoff, d.sx, d.xsoff + kb * 1024);
  else dma16_sc1(d.m0x + bo + 1024, d.xoff, d.sx, d.xsoff + kb2 * 1024);
}

DI void gemm1_body(u32 sb, srd_t sw, srd_t sx, u32 wsoff, u32 xsoff, int w, int lane, int wm, int wn,
                   char* ws, u32 hoffA, u32* hsig, const G2Dma& g2, const u32* hflg, v4u (&hB)[4]) {
  constexpr int NU = 8;
  const int g = lane >> 4, qq = (lane & 15) >> 2, p = lane & 3;
  const int rr = 4 * (g & 1) + qq;
  const int wl0 = rr * 128 + (2 * (rr >> 1)) * 16 + p * 8;
  const u32 woff = (u32)((lane >> 3) * (F_ * 2) + ((lane & 7) ^ (2 * (lane >> 4))) * 16);
  const u32 xoff = lane * 16;
  v8s wf[3], xf[2][4];
  const u32 m0wB = sb + (w & 3) * 2048 + (w >> 2) * 1024;
  const u32 m0xB = sb + 16384 + w * 4096;
  auto dmaB = [&](int st, int i) {
    const u32 bo = b_slot(st);
    if (i < 2) dma16(m0wB + bo + i * 8192, woff, sw, wsoff + (u32)st * (64 * F_ * 2) + 256 + i * (32 * F_ * 2));
    else dma16(m0xB + bo + (i - 2) * 1024, xoff, sx, xsoff + st * 2048 + ((i - 2) >> 1) * (18 * 1024) + ((i - 2) & 1) * 1024);
  };
  v4f accA[2][4][4];
  {
    u32 wp[4], xp;
#pragma unroll
    for (int t = 0; t < 4; ++t) wp[t] = opaque((sb + wl0 + (g >> 1) * 4096 + wn * 1024) ^ (t << 5));
    xp = opaque(sb + 16384 + wm * 4096 + lane * 16);
    const u32 m0w = sb + (w & 3) * 4096 + (w >> 2) * 2048;
    const u32 m0x = sb + 16384 + w * 2048;
    auto dma_piece = [&](int st, int i) {
      const u32 bo = (st & 3) * A_STAGE;
      if (i < 2) dma16(m0w + bo + i * 1024, woff, sw, wsoff + (u32)st * (32 * F_ * 2) + i * 128);
      else dma16(m0x + bo + (i - 2) * 1024, xoff, sx, xsoff + st * 1024 + (i - 2) * (18 * 1024));
    };
#pragma unroll
    for (int st = 0; st < 3; ++st)
#pragma unroll
      for (int i = 0; i < 4; ++i) dma_piece(st, i);
    wait_vm<8>();
    stage_barrier();
#pragma unroll
    for (int mb = 0; mb < 4; ++mb) xf[0][mb] = ld_frag(xp, mb * 1024);
    wf[0] = tr_frag(wp[0], 0, 8192);
    wf[1] = tr_frag(wp[0], 2048, 2048 + 8192);
    dma_piece(3, 0); dma_piece(3, 1);
    SCHED0();
    static_for<18 * NU>([&](auto UC) {
      constexpr int U = decltype(UC)::value, st = U / NU, u = U % NU;
      constexpr int T = u >> 1, gu = u & 1;
      constexpr bool trans = (u == NU - 2 && st + 1 < 18);
      constexpr bool rd = (u + 2 < NU) || (st + 1 < 18);
      constexpr int un = (u + 2) % NU;
      auto mm = [&](int mb) {
        if constexpr (st == 0) mfma16_first(accA[gu][T][mb], wf[U % 3], xf[st & 1][mb]);
        else mfma16(accA[gu][T][mb], wf[U % 3], xf[st & 1][mb]);
        SCHED0();
      };
      v4s lo, hi;
      mm(0);
      if constexpr (!trans && rd) { lo = tr_half(wp[un >> 1], (un & 1) * 2048); SCHED0(); }
      mm(1);
      if constexpr (trans) {
        if constexpr (st + 1 <= 15) wait_vm<8>(); else if constexpr (st + 1 == 16) wait_vm<4>(); else wait_vm<8>();
        stage_barrier();
        constexpr int dl = (((st + 1) & 3) == 0) ? -3 * A_STAGE : A_STAGE;
#pragma unroll
        for (int t = 0; t < 4; ++t) wp[t] = opaque(wp[t] + dl);
        xp = opaque(xp + dl);
        lo = tr_half(wp[un >> 1], (un & 1) * 2048);
      }
      if constexpr (rd) { hi = tr_half(wp[un >> 1], (un & 1) * 2048 + 8192); wf[(U + 2) % 3] = join(lo, hi); }
      SCHED0();
      mm(2);
      if constexpr (u >= NU - 2 && st + 1 < 18) {
        xf[(st + 1) & 1][2 * (u - 6)] = ld_frag(xp, (2 * (u - 6)) * 1024);
        xf[(st + 1) & 1][2 * (u - 6) + 1] = ld_frag(xp, (2 * (u - 6) + 1) * 1024);
      }
      SCHED0();
      mm(3);
      if constexpr (u >= NU - 2) { if constexpr (st + 4 < 18) dma_piece(st + 4, u - (NU - 2)); }
      else if constexpr (u < 2) { if constexpr (st + 3 < 18) dma_piece(st + 3, u + 2); }
      constexpr int qB = U - (15 * NU + 6);
      if constexpr (qB >= 0 && qB < 12) dmaB(qB / 6, qB % 6);
      SCHED0();
    });
  }
  {
    v4f acc[2][2][4];
    v4u hA[8];
    u32 wp[2], xp, pollA = 0;
#pragma unroll
    for (int t = 0; t < 2; ++t) wp[t] = opaque((sb + b_slot(0) + wl0 + (g >> 1) * 2048) ^ ((2 * wn + t) << 5));
    xp = opaque(sb + b_slot(0) + 16384 + wm * 8192 + lane * 16);
    auto storeA = [&](int i) {
      __builtin_amdgcn_raw_buffer_store_b128(hA[i], g2.sx, lane * 16, hoffA + (i & 3) * (144 * 1024) + (i >> 2) * 1024, 16);
    };
    auto conv1 = [&](int cq) {
      const int gi = cq >> 2, pr = gi >> 2, mb = gi & 3, cT = 2 * pr + ((cq >> 1) & 1), ch = cq & 1;
      hA[gi][cq & 3] = h_dword(accA[0][cT][mb][2 * ch], accA[0][cT][mb][2 * ch + 1], accA[1][cT][mb][2 * ch], accA[1][cT][mb][2 * ch + 1]);
    };
    wait_vm<6>();
    stage_barrier();
#pragma unroll
    for (int mb = 0; mb < 4; ++mb) xf[0][mb] = ld_frag(xp, mb * 2048);
    wf[0] = tr_frag(wp[0], 0, 4096);
    wf[1] = tr_frag(wp[0], 1024, 1024 + 4096);
    dmaB(2, 0); dmaB(2, 1);
    mfma_drain();
    SCHED0();
    static_for<9 * NU>([&](auto UC) {
      constexpr int U = decltype(UC)::value, st = U / NU, u = U % NU;
      constexpr int T = (u >> 1) & 1, gu = u & 1;
      constexpr bool trans = (u == NU - 2 && st + 1 < 9);
      constexpr bool rd = (u + 2 < NU) || (st + 1 < 9);
      constexpr int un = (u + 2) % NU;
      constexpr int wo = (un >> 2) * 8192 + (un & 1) * 1024;
      auto mm = [&](int mb) {
        if constexpr (st == 0 && u < 4) mfma16_first(acc[gu][T][mb], wf[U % 3], xf[u >> 2][mb]);
        else mfma16(acc[gu][T][mb], wf[U % 3], xf[u >> 2][mb]);
        SCHED0();
      };
      v4s lo, hi;
      mm(0);
      if constexpr (!trans && rd) { lo = tr_half(wp[(un >> 1) & 1], wo); }
      if constexpr (U < 16) conv1(2 * U);
      SCHED0();
      mm(1);
      if constexpr (trans) {
        wait_vm<gb_wait(st)>();
        stage_barrier();
        if constexpr (st == 4) h_signal(hsig, lane);
        if constexpr (st == 6) poll_issue(pollA, hflg);
        if constexpr (st == 7) { h_signal(hsig + 64 * 32, lane); flags_check(pollA, hflg); }
        constexpr int dl = b_slot(st + 1) - b_slot(st);
#pragma unroll
        for (int t = 0; t < 2; ++t) wp[t] = opaque(wp[t] + dl);
        xp = opaque(xp + dl);
        lo = tr_half(wp[(un >> 1) & 1], wo);
      }
      if constexpr (rd) { hi = tr_half(wp[(un >> 1) & 1], wo + 2 * 2048); wf[(U + 2) % 3] = join(lo, hi); }
      SCHED0();
      mm(2);
      if constexpr (u == 1 || u == 2) { xf[1][2 * (u - 1)] = ld_frag(xp, (2 * (u - 1)) * 2048 + 1024); xf[1][2 * (u - 1) + 1] = ld_frag(xp, (2 * (u - 1) + 1) * 2048 + 1024); }
      if constexpr (u >= NU - 2 && st + 1 < 9) { xf[0][2 * (u - 6)] = ld_frag(xp, (2 * (u - 6)) * 2048); xf[0][2 * (u - 6) + 1] = ld_frag(xp, (2 * (u - 6) + 1) * 2048); }
      if constexpr (U < 16) conv1(2 * U + 1);
      SCHED0();
      mm(3);
      if constexpr (u >= NU - 2) { if constexpr (st + 3 < 9) dmaB(st + 3, u - (NU - 2)); }
      else if constexpr (u < 4) { if constexpr (st + 2 < 9) dmaB(st + 2, u + 2); }
      if constexpr ((st == 1 || st == 2) && (u == 4 || u == 5)) storeA(2 * (st - 1) + u - 4);
      if constexpr (st >= 3 && st <= 6 && u == 4) storeA(st + 1);
      constexpr int qG = U - (7 * NU + 6);
      if constexpr (qG >= 0 && qG < 4) { g2_piece(g2, lane, (2 * qG) / 3, (2 * qG) % 3); g2_piece(g2, lane, (2 * qG + 1) / 3, (2 * qG + 1) % 3); }
      if constexpr (qG == 4) g2_piece(g2, lane, 2, 2);
      if constexpr (qG >= 5 && qG < 8) { g2_piece(g2, lane, qG - 5, 3); g2_piece(g2, lane, qG - 5, 4); }
      SCHED0();
    });
    mfma_drain();
    SCHED0();
#pragma unroll
    for (int mb = 0; mb < 4; ++mb)
#pragma unroll
      for (int c = 0; c < 4; ++c)
        hB[mb][c] = h_dword(acc[0][c >> 1][mb][2 * (c & 1)], acc[0][c >> 1][mb][2 * (c & 1) + 1],
                            acc[1][c >> 1][mb][2 * (c & 1)], acc[1][c >> 1][mb][2 * (c & 1) + 1]);
  }
}

template <int NT, bool PRE>
DI void gemm2_body(char* smem, u32 sb, const G2Dma& g2, int w, int lane, int kg, int wm, int tbase, int tile, int ksl,
                   const u16* __restrict__ xin, u16* __restrict__ out, char* ws, u32* hsigB, const v4u (&hB)[4],
                   u32 hBoff) {
  constexpr int NU = NT;
  constexpr int NO = (2 * NT + 3) / 4;
  v4f acc[NT][4];
  const int g = lane >> 4, qq = (lane & 15) >> 2, p = lane & 3;
  const int rr = 4 * (g & 1) + qq;
  const int wl0 = rr * 128 + (2 * (rr >> 1)) * 16 + p * 8;
  u32 wp[NT], xp;
#pragma unroll
  for (int t = 0; t < 4; ++t)
    wp[t] = opaque((sb + g_slot(0) + wl0 + ((g >> 1) + 4 * kg) * 2304 + (NT == 5 ? 1024 : 0)) ^ (t << 5));
  if constexpr (NT == 5) wp[4] = opaque(sb + g_slot(0) + 2048 + rr * 32 + p * 8 + ((g >> 1) + 4 * kg) * 2304);
  xp = opaque(sb + g_slot(0) + G_XOFF + wm * 8192 + kg * 1024 + lane * 16);
  auto rdW = [&](int u) -> v8s { return tr_frag(wp[u], 0, 2 * 2304); };
  const u32* hflg = (const u32*)(ws + WS_HCNT) + ((tile >> 2) * 4 + ksl) * 32 + (lane < 24 ? lane : 0);
  u32* ycnt = (u32*)(ws + WS_YCNT);
  v8s wf[3], xf[2][4];
  u32 pollB = 0, pollY = 0;
  const int j = 2 * wm + kg;
  const int tok0 = (tile >> 2) * 128 + j * 32 + (lane & 15) + 16 * (ksl & 1);
  const int dcol = (tile & 3) * 144 + tbase * 16 + (lane >> 4) * 4 + 16 * (ksl >> 1);
  v2u xv[NO]; u32 yv[NO][4]; v2f stt;
  constexpr int NL = 1 + 5 * NO;
  char* xw = smem + ((wm * 2 + kg) * 9 + tbase) * 2048 + lane * 16;
  if constexpr (!PRE) {
#pragma unroll
    for (int st = 0; st < 3; ++st)
#pragma unroll
      for (int i = 0; i < 3; ++i) g2_piece(g2, lane, st, i);
    wait_flags(hflg);
#pragma unroll
    for (int st = 0; st < 3; ++st) { g2_piece(g2, lane, st, 3); g2_piece(g2, lane, st, 4); }
  }
  wait_vm<4>();
  stage_barrier();
#pragma unroll
  for (int mb = 0; mb < 4; ++mb) xf[0][mb] = ld_frag(xp, mb * 2048);
  wf[0] = rdW(0);
  wf[1] = rdW(1);
  g2_piece(g2, lane, 3, 0); g2_piece(g2, lane, 3, 1);
  SCHED0();
  static_for<18 * NU>([&](auto UC) {
    constexpr int U = decltype(UC)::value, st = U / NU, u = U % NU;
    constexpr int SB = PRE ? 2 : 0;
    constexpr bool trans = (u == NU - 2 && st + 1 < 18);
    constexpr bool rd = (u + 2 < NU) || (st + 1 < 18);
    constexpr int un = (u + 2) % NU;
    auto mm = [&](int mb) {
      if constexpr (st == 0) mfma16_first(acc[u][mb], wf[U % 3], xf[st & 1][mb]);
      else mfma16(acc[u][mb], wf[U % 3], xf[st & 1][mb]);
      SCHED0();
    };
    v4s lo, hi;
    mm(0);
    if constexpr (!trans && rd) { lo = tr_half(wp[un], 0); SCHED0(); }
    mm(1);
    if constexpr (trans) {
      constexpr int NW = st == 0 ? 7 + SB : st == 1 ? 11 + 2 * SB : st == 2 ? 10 + SB : st == 3 ? 10 + SB
                         : st + 1 <= 15 ? 10 : st + 1 == 16 ? 5 : 0;
      wait_vm<NW>();
      stage_barrier();
      if constexpr (st == 0) poll_issue(pollB, hflg + 64 * 32);
      if constexpr (st == 2) flags_check(pollB, hflg + 64 * 32);
      if constexpr (PRE && st == 4) h_signal(hsigB, lane);
      if constexpr (st == 6) poll_issue(pollB, hflg + 128 * 32);
      if constexpr (st == 8) flags_check(pollB, hflg + 128 * 32);
      if constexpr (st == 12) poll_issue(pollY, ycnt);
      constexpr int dl = g_slot(st + 1) - g_slot(st);
#pragma unroll
      for (int t = 0; t < NT; ++t) wp[t] = opaque(wp[t] + dl);
      xp = opaque(xp + dl);
      lo = tr_half(wp[un], 0);
    }
    if constexpr (rd) { hi = tr_half(wp[un], 2 * 2304); wf[(U + 2) % 3] = join(lo, hi); }
    SCHED0();
    mm(2);
    if constexpr (u >= NU - 2 && st + 1 < 18) {
      xf[(st + 1) & 1][2 * (u - (NU - 2))] = ld_frag(xp, (2 * (u - (NU - 2))) * 2048);
      xf[(st + 1) & 1][2 * (u - (NU - 2)) + 1] = ld_frag(xp, (2 * (u - (NU - 2)) + 1) * 2048);
    }
    if constexpr (u == 0) { if constexpr (st + 3 < 18) g2_piece(g2, lane, st + 3, 2); }
    SCHED0();
    mm(3);
    if constexpr (u >= NU - 2) { if constexpr (st + 4 < 18) g2_piece(g2, lane, st + 4, u - (NU - 2)); }
    else if constexpr (u == 0) { if constexpr (st + 3 < 18) g2_piece(g2, lane, st + 3, 3); }
    else if constexpr (u == 1) { if constexpr (st + 3 < 18) g2_piece(g2, lane, st + 3, 4); }
    if constexpr (PRE && u == 1 && st < 2) {
#pragma unroll
      for (int mb = 2 * st; mb < 2 * st + 2; ++mb)
        __builtin_amdgcn_raw_buffer_store_b128(hB[mb], g2.sx, lane * 16, hBoff + mb * (144 * 1024), 16);
    }
    if constexpr (st == 17 && u >= 1) {
#pragma unroll
      for (int b2 = 0; b2 < 2; ++b2) *(v4f*)(xw + ((u - 1) * 2 + b2) * 1024) = kg ? acc[u - 1][b2] : acc[u - 1][2 + b2];
    }
    SCHED0();
  });
  mfma_drain();
  SCHED0();
#pragma unroll
  for (int b2 = 0; b2 < 2; ++b2) *(v4f*)(xw + ((NT - 1) * 2 + b2) * 1024) = kg ? acc[NT - 1][b2] : acc[NT - 1][2 + b2];
  stage_barrier();
  v4f r[2 * NT];
  {
    const char* rd = smem + ((wm * 2 + (kg ^ 1)) * 9 + tbase) * 2048 + lane * 16;
#pragma unroll
    for (int T = 0; T < NT; ++T)
#pragma unroll
      for (int b2 = 0; b2 < 2; ++b2) {
        const v4f o = *(const v4f*)(rd + (T * 2 + b2) * 1024);
        r[T * 2 + b2] = kg ? (o + acc[T][2 + b2]) : (acc[T][b2] + o);
      }
  }
  const srd_t sp = make_srd(ws + WS_PART, 64 * 4 * 8 * 4 * 3072);
#pragma unroll
  for (int f = 0; f < 2 * NT; ++f)
    if ((f & 3) != ksl)
      __builtin_amdgcn_raw_buffer_store_b128(__builtin_bit_cast(v4u, r[f]), sp, lane * 16,
                                             (u32)((((tile * 4 + (f & 3)) * 8 + w) * 4 + ksl) * 3072 + (f >> 2) * 1024), 16);
  v4f own[NO];
#pragma unroll
  for (int i = 0; i < NO; ++i) {
    const v4f a0 = r[4 * i], a1 = r[4 * i + 1];
    const v4f a2 = (4 * i + 2 < 2 * NT) ? r[(4 * i + 2 < 2 * NT) ? 4 * i + 2 : 0] : a0;
    const v4f a3 = (4 * i + 3 < 2 * NT) ? r[(4 * i + 3 < 2 * NT) ? 4 * i + 3 : 0] : a0;
    own[i] = ksl == 0 ? a0 : ksl == 1 ? a1 : ksl == 2 ? a2 : a3;
  }
  SCHED0();
  poll_check(pollY, ycnt, 576u);
  {
    const srd_t sy = make_srd(ws + WS_YT, S_ * D_ * 2);
    stt = ((const v2f*)(ws + WS_STATS))[tok0];
#pragma unroll
    for (int i = 0; i < NO; ++i) {
      const int dc = (4 * i + ksl < 2 * NT) ? dcol + 32 * i : dcol;
      xv[i] = *(const v2u*)(xin + (size_t)tok0 * D_ + dc);
#pragma unroll
      for (int r = 0; r < 4; ++r) yv[i][r] = __builtin_amdgcn_raw_buffer_load_b16(sy, (u32)(((dc + r) * S_ + tok0) * 2), 0, 16);
    }
  }
  SCHED0();
  wait_vm<NL>();
  u32* flags = (u32*)(ws + WS_CNT);
  if (lane < 4 && lane != ksl)
    __hip_atomic_store(flags + ((tile * 4 + lane) * 8 + w) * 16 + ksl, 1u, __ATOMIC_RELAXED, __HIP_MEMORY_SCOPE_AGENT);
  float xa[NO][4];
#pragma unroll
  for (int i = 0; i < NO; ++i)
#pragma unroll
    for (int e = 0; e < 4; ++e) {
      const float xf_ = (e & 1) ? bf_hi(xv[i][e >> 1]) : bf_lo(xv[i][e >> 1]);
      const float xn = rbf(rbf(xf_ - stt[0]) * stt[1]);
      xa[i][e] = rbf(xf_ + rbf(xn * bf_lo(yv[i][e])));
    }
  {
    u32* fl = flags + ((tile * 4 + ksl) * 8 + w) * 16;
    u32 spins = 0;
    for (;;) {
      const u32 v = (lane < 4 && lane != ksl) ? __hip_atomic_load(fl + lane, __ATOMIC_RELAXED, __HIP_MEMORY_SCOPE_AGENT) : 1u;
      if (__all(v != 0u)) break;
      __builtin_amdgcn_s_sleep(1);
      if (++spins > (1u << 24)) break;
    }
  }
  __builtin_amdgcn_fence(__ATOMIC_ACQUIRE, "wavefront");
  v4f pp[3][NO];
#pragma unroll
  for (int i3 = 0; i3 < 3; ++i3) {
    const int src = i3 + (i3 >= ksl ? 1 : 0);
    const u32 po = (u32)((((tile * 4 + ksl) * 8 + w) * 4 + src) * 3072);
#pragma unroll
    for (int i = 0; i < NO; ++i)
      pp[i3][i] = __builtin_bit_cast(v4f, __builtin_amdgcn_raw_buffer_load_b128(sp, lane * 16, po + i * 1024, 16));
  }
#pragma unroll
  for (int i = 0; i < NO; ++i) {
    const v4f s0 = ksl == 0 ? own[i] : pp[0][i];
    const v4f s1 = ksl == 1 ? own[i] : (ksl < 1 ? pp[0][i] : pp[1][i]);
    const v4f s2 = ksl == 2 ? own[i] : (ksl < 2 ? pp[1][i] : pp[2][i]);
    const v4f s3 = ksl == 3 ? own[i] : pp[2][i];
    const v4f B = (s0 + s1) + (s2 + s3);
    const float o0 = xa[i][0] + rbf(B[0]), o1 = xa[i][1] + rbf(B[1]);
    const float o2 = xa[i][2] + rbf(B[2]), o3 = xa[i][3] + rbf(B[3]);
    if (4 * i + ksl < 2 * NT)
      *(v2u*)(out + (size_t)tok0 * D_ + dcol + 32 * i) = v2u{pack_bf(o0, o1), pack_bf(o2, o3)};
  }
}

__global__ __launch_bounds__(512, 2) void fused_kernel(const u16* __restrict__ Wg, const u16* __restrict__ Wu,
                                                       const u16* __restrict__ Wd, const u16* __restrict__ ssmk,
                                                       const u16* __restrict__ ssmD, const u16* __restrict__ xin,
                                                       u16* __restrict__ out, char* __restrict__ ws) {
  extern __shared__ __attribute__((aligned(1024))) char smem[];
  const int bid = blockIdx.x;
  const int xcd = bid & 7, idx = bid >> 3;
  const int tid = threadIdx.x, lane = tid & 63;
  const int w = __builtin_amdgcn_readfirstlane(tid >> 6);
  const int ksl = xcd >> 1;
  const bool isconv = idx >= 24;
  const int tile2 = isconv ? (2 * (idx - 24) + (xcd & 1)) * 4 + 3 : (2 * (idx / 3) + (xcd & 1)) * 4 + idx % 3;
  const u32 sb = lds_addr(smem);
  G2Dma g2;
  g2.sw = make_srd(Wd, D_ * F_ * 2);
  g2.sx = make_srd(ws + WS_HT, S_ * F_ * 2);
  g2.wsoffA = (u32)((ksl * 1152 + 8 * (w & 3) + (w >> 2) * 64) * (D_ * 2) + (tile2 & 3) * 288);
  g2.wsoffB = (u32)((ksl * 1152 + 8 * w) * (D_ * 2) + (tile2 & 3) * 288);
  g2.xsoff = (u32)((((tile2 >> 2) * 8 + w) * 144 + ksl * 36) * 1024);
  g2.m0w = sb + w * 2304;
  g2.m0x = sb + G_XOFF + w * 2048;
  g2.woff = (u32)((lane >> 3) * (D_ * 2) + ((lane & 7) ^ (2 * (lane >> 4))) * 16);
  g2.soff = (u32)(((lane >> 1) & 7) * (D_ * 2) + (lane & 1) * 16);
  g2.xoff = lane * 16;
  const int kg2 = w >> 2, wm2 = (w >> 1) & 1, wn2 = (w & 1) ^ kg2;
  v4u hB[4];
  if (isconv) {
    const int cb = xcd * 8 + (idx - 24);
    if (w == 7) {
      const int c8 = idx - 24;
      u32 t[42];
#pragma unroll
      for (int i = 0; i < 14; ++i) {
        int e = (i * 8 + c8) * 64 + lane;
        e = e < 6912 ? e : 0;
        const int row = e / 12, r12 = e % 12, mtx = r12 / 6, l6 = r12 % 6;
        t[i] = *(const u32*)((const char*)(mtx ? Wu : Wg) + (size_t)row * (F_ * 2) + xcd * 1152 + (l6 >> 1) * 384 + (l6 & 1) * 128);
      }
#pragma unroll
      for (int i = 0; i < 7; ++i) {
        int e = (i * 8 + c8) * 64 + lane;
        e = e < 3456 ? e : 0;
        const int row = e / 6, r6 = e % 6, mtx = r6 / 3, l3 = r6 % 3;
        t[14 + i] = *(const u32*)((const char*)(mtx ? Wu : Wg) + (size_t)row * (F_ * 2) + xcd * 1152 + l3 * 384 + 256);
      }
#pragma unroll
      for (int i = 0; i < 21; ++i) {
        int e = (i * 8 + c8) * 64 + lane;
        e = e < 10368 ? e : 0;
        t[21 + i] = *(const u32*)((const char*)Wd + (size_t)ksl * (1152 * 1152) + (size_t)e * 128);
      }
      u32 a = 0;
#pragma unroll
      for (int i = 0; i < 42; ++i) a ^= t[i];
      asm volatile("" :: "v"(a));
    }
#pragma unroll 1
    for (int c = w; c < 9 && w < 7; c += 7) {
      conv_channel(smem + w * C_WAVE, cb * 9 + c, lane, ssmk, ssmD, ws);
    }
    stage_barrier();
#pragma unroll
    for (int i = 0; i < 4; ++i) hB[i] = v4u{0u, 0u, 0u, 0u};
    if (wn2 == 0) gemm2_body<5, false>(smem, sb, g2, w, lane, kg2, wm2, 4, tile2, ksl, xin, out, ws, nullptr, hB, 0u);
    else          gemm2_body<4, false>(smem, sb, g2, w, lane, kg2, wm2, 0, tile2, ksl, xin, out, ws, nullptr, hB, 0u);
  } else {
    const int tile_n = xcd * 3 + idx % 3, tile_m = idx / 3;
    const int wm = w & 3, wn = w >> 2;
    const srd_t sw = make_srd((w >> 2) ? Wu : Wg, D_ * F_ * 2);
    const srd_t sx = make_srd(ws + WS_XNT, S_ * D_ * 2);
    const u32 wsoff = (u32)((8 * (w & 3)) * (F_ * 2) + tile_n * 384);
    const u32 xsoff = (u32)((tile_m * 16 + 2 * w) * (18 * 1024));
    u32* hsigA = (u32*)(ws + WS_HCNT) + ((2 * tile_m + (wm >> 1)) * 4 + ksl) * 32 + (tile_n % 6) * 4 + (wm & 1) * 2 + wn;
    const u32* hflg = (const u32*)(ws + WS_HCNT) + ((tile2 >> 2) * 4 + ksl) * 32 + (lane < 24 ? lane : 0);
    const u32 hoff = (u32)(((tile_m * 16 + wm * 4) * 144 + tile_n * 6) * 1024);
    gemm1_body(sb, sw, sx, wsoff, xsoff, w, lane, wm, wn, ws, hoff + 2 * wn * 1024, hsigA, g2, hflg, hB);
    const u32 hBoff = hoff + (4 + wn) * 1024;
    if (wn2 == 0) gemm2_body<5, true>(smem, sb, g2, w, lane, kg2, wm2, 4, tile2, ksl, xin, out, ws, hsigA + 128 * 32, hB, hBoff);
    else          gemm2_body<4, true>(smem, sb, g2, w, lane, kg2, wm2, 0, tile2, ksl, xin, out, ws, hsigA + 128 * 32, hB, hBoff);
  }
}

extern "C" void kernel_launch(void* const* d_in, const int* in_sizes, int n_in, void* d_out, int out_size,
                              void* d_ws, size_t ws_size, hipStream_t stream) {
  const u16* q = (const u16*)d_in[0];
  const u16* k = (const u16*)d_in[1];
  const u16* v = (const u16*)d_in[2];
  const u16* shk = (const u16*)d_in[3];
  const u16* ssmk = (const u16*)d_in[4];
  const u16* ssmD = (const u16*)d_in[5];
  const u16* Wg = (const u16*)d_in[7];
  const u16* Wu = (const u16*)d_in[8];
  const u16* Wd = (const u16*)d_in[9];
  char* ws = (char*)d_ws;
  (void)hipFuncSetAttribute((const void*)fused_kernel, hipFuncAttributeMaxDynamicSharedMemorySize, H_LDS);
  pre_kernel<<<256 + 288, 256, 0, stream>>>(q, k, v, shk, ws);
  fused_kernel<<<256, 512, H_LDS, stream>>>(Wg, Wu, Wd, ssmk, ssmD, q, (u16*)d_out, ws);
}
```
